```python
import torch

def _softmax(x, axis=-1):
    return torch.softmax(x, dim=axis)
def _gelu(x):
    return torch.nn.functional.gelu(x, approximate="tanh")
def _silu(x):
    return torch.nn.functional.silu(x)
def _sigmoid(x):
    return torch.sigmoid(x)
def _layernorm(x, axis=-1, eps=1e-5):
    m = x.mean(dim=axis, keepdim=True)
    v = x.var(dim=axis, keepdim=True, unbiased=False)
    return (x - m) / torch.sqrt(v + eps)
def _scatter_add(tgt, idx, src, axis=0):
    return tgt.clone().index_add_(axis, idx, src)
def _segment_sum(src, seg, n):
    out = torch.zeros((n,) + src.shape[1:], dtype=src.dtype)
    return out.index_add_(0, seg, src)
def _pad(x, pw):
    flat = tuple(v for pair in reversed(pw) for v in pair)
    return torch.nn.functional.pad(x, flat)

kernel_name = 'sequential_preLN__ssm_conv_h3__dense_activation_silu__prompt'

def setup_inputs():

    _w = (lambda a: (lambda t: t.to(torch.bfloat16) if t.is_floating_point() else t)(torch.as_tensor(a)))
    def _A_setup():
        B, S, D = 1, 2048, 512
        ks = 4
        return {
            "q": _w((lambda *s: torch.randn(*s, dtype=torch.bfloat16) * 0.05)(B, S, D)),
            "k": _w((lambda *s: torch.randn(*s, dtype=torch.bfloat16) * 0.2)(B, S, D)),
            "v": _w((lambda *s: torch.randn(*s, dtype=torch.bfloat16) * 0.2)(B, S, D)),
            "shift_k": _w((lambda *s: torch.randn(*s, dtype=torch.bfloat16) * 0.2)(D, ks) * 0.5),
            "ssm_k": _w((lambda *s: torch.randn(*s, dtype=torch.bfloat16) * 0.4)(D, S) * (1.0 / S ** 0.5)),
            "ssm_D": _w((lambda *s: torch.randn(*s, dtype=torch.bfloat16) * 0.2)(D)),
        }
    _A_in = _A_setup()

    def _B_setup():
        B, S, D, F = 1, 2048, 512, 1792
        return {
            "x": _w((lambda *s: torch.randn(*s, dtype=torch.bfloat16) * 0.05)(B, S, D)),
            "w1": _w((lambda *s: torch.randn(*s, dtype=torch.bfloat16) * 0.8)(D, F) * (D ** -0.5)),
            "w2": _w((lambda *s: torch.randn(*s, dtype=torch.bfloat16) * 0.1)(F, D) * (F ** -0.5)),
        }
    _B_in = _B_setup()

    return {
        "A_q": _A_in["q"],
        "A_k": _A_in["k"],
        "A_v": _A_in["v"],
        "A_shift_k": _A_in["shift_k"],
        "A_ssm_k": _A_in["ssm_k"],
        "A_ssm_D": _A_in["ssm_D"],
        "B_x": _B_in["x"],
        "B_w1": _B_in["w1"],
        "B_w2": _B_in["w2"],
    }

def reference(A_q, A_k, A_v, A_shift_k, A_ssm_k, A_ssm_D, B_x, B_w1, B_w2):
    def _A_ref(q, k, v, shift_k, ssm_k, ssm_D):
        B, S, D = q.shape
        ks = shift_k.shape[-1]
        k_pad = _pad(k, ((0, 0), (ks - 1, 0), (0, 0)))
        k_shift = torch.zeros_like(k)
        for t in range(ks):
            k_shift = k_shift + k_pad[:, t:t + S, :] * shift_k[:, ks - 1 - t][None, None, :]
        kv = k_shift * v
        L = 2 * S
        kv_pad = _pad(kv, ((0, 0), (0, S), (0, 0)))
        kf_pad = _pad(ssm_k, ((0, 0), (0, S)))
        kv_f = (lambda x, axes=(-2,-1): torch.fft.rfft2(x.float(), dim=axes))(kv_pad[:, None, :, :], axes=(1, 2))
        kf_f = (lambda x, axes=(-2,-1): torch.fft.rfft2(x.float(), dim=axes))(kf_pad[:, None, :], axes=(1, 2))
        kf_f = (lambda x, *d: x.permute(*d))(kf_f, 1, 2, 0)
        y_f = kv_f * kf_f[None, :, :, :]
        y = (lambda x, s, axes=(-2,-1): torch.fft.irfft2(x, s=s, dim=axes).to(torch.bfloat16))(y_f, (1, L), axes=(1, 2))[:, 0, :S, :]
        y = y + kv * ssm_D[None, None, :]
        return q * y

    def _B_ref(x, w1, w2):
        h = torch.einsum("bsd,df->bsf", x, w1)
        h = _silu(h)
        out = torch.einsum("bsf,fd->bsd", h, w2)
        return out

    x = A_q
    h = x + _A_ref(_layernorm(x, axis=-1), A_k, A_v, A_shift_k, A_ssm_k, A_ssm_D)
    h2 = h + _B_ref(_layernorm(h, axis=-1), B_w1, B_w2)
    return h2
```

```cpp
#include <hip/hip_runtime.h>
#include <hip/hip_bf16.h>
#include <stdint.h>
typedef unsigned short u16;
static __device__ __forceinline__ float bf2f(u16 b){ return __uint_as_float(((unsigned)b)<<16); }
static __device__ __forceinline__ u16 f2bf(float f){ __hip_bfloat16 h = __float2bfloat16(f); return *reinterpret_cast<u16*>(&h); }
static __device__ __forceinline__ float rb(float f){ return bf2f(f2bf(f)); }
constexpr int S=2048, D=512, F=1792;

__global__ void k_kv(const u16* k, const u16* v, const u16* sk, u16* kv){
  int i = blockIdx.x*256+threadIdx.x; int s=i/D, d=i%D;
  float acc=0.f;
  for(int t=0;t<4;t++){ int r=s+t-3; float kk = r>=0? bf2f(k[r*D+d]):0.f; acc = rb(acc + rb(kk*bf2f(sk[d*4+3-t]))); }
  kv[i]=f2bf(acc*bf2f(v[i]));
}
__global__ void k_conv(const u16* kv, const u16* g, const u16* Dv, u16* y){
  int i = blockIdx.x*256+threadIdx.x; int s=i/D, d=i%D;
  float acc=0.f;
  for(int j=0;j<=s;j++) acc += bf2f(kv[(s-j)*D+d])*bf2f(g[d*S+j]);
  float yy = rb(acc);
  y[i]=f2bf(yy + rb(bf2f(kv[i])*bf2f(Dv[d])));
}
__device__ void ln_stats(const float* x8, float& m, float& dn){
  float s=0; for(int j=0;j<8;j++) s+=x8[j];
  for(int o=32;o;o>>=1) s+=__shfl_xor(s,o);
  float mean=s/D; float q=0; for(int j=0;j<8;j++){float t=x8[j]-mean; q+=t*t;}
  for(int o=32;o;o>>=1) q+=__shfl_xor(q,o);
  m=rb(mean); float var=rb(q/D); dn=rb(sqrtf(rb(var+1e-5f)));
}
__global__ void k_h(const u16* q, const u16* y, u16* h, u16* hn){
  int s=blockIdx.x; int l=threadIdx.x; float x[8];
  for(int j=0;j<8;j++) x[j]=bf2f(q[s*D+l*8+j]);
  float m,dn; ln_stats(x,m,dn);
  float hh[8];
  for(int j=0;j<8;j++){ float qn=rb(rb(x[j]-m)/dn); hh[j]=rb(x[j]+rb(qn*bf2f(y[s*D+l*8+j]))); h[s*D+l*8+j]=f2bf(hh[j]); }
  ln_stats(hh,m,dn);
  for(int j=0;j<8;j++) hn[s*D+l*8+j]=f2bf(rb(hh[j]-m)/dn);
}
__global__ void k_g1(const u16* hn, const u16* w1, u16* G){
  int i = blockIdx.x*256+threadIdx.x; int s=i/F, f=i%F;
  float acc=0; for(int d=0;d<D;d++) acc+=bf2f(hn[s*D+d])*bf2f(w1[d*F+f]);
  float g=rb(acc); G[i]=f2bf(g/(1.f+__expf(-g)));
}
__global__ void k_g2(const u16* G, const u16* w2, const u16* h, u16* out){
  int i = blockIdx.x*256+threadIdx.x; int s=i/D, d=i%D;
  float acc=0; for(int f=0;f<F;f++) acc+=bf2f(G[s*F+f])*bf2f(w2[f*D+d]);
  out[i]=f2bf(bf2f(h[i])+rb(acc));
}
extern "C" void kernel_launch(void* const* d_in, const int* in_sizes, int n_in, void* d_out, int out_size, void* d_ws, size_t ws_size, hipStream_t stream){
  const u16* q=(const u16*)d_in[0]; const u16* k=(const u16*)d_in[1]; const u16* v=(const u16*)d_in[2];
  const u16* sk=(const u16*)d_in[3]; const u16* g=(const u16*)d_in[4]; const u16* Dv=(const u16*)d_in[5];
  const u16* w1=(const u16*)d_in[7]; const u16* w2=(const u16*)d_in[8];
  u16* ws=(u16*)d_ws; u16* kv=ws; u16* y=kv+S*D; u16* h=y+S*D; u16* hn=h+S*D; u16* G=hn+S*D;
  k_kv<<<S*D/256,256,0,stream>>>(k,v,sk,kv);
  k_conv<<<S*D/256,256,0,stream>>>(kv,g,Dv,y);
  k_h<<<S,64,0,stream>>>(q,y,h,hn);
  k_g1<<<S*F/256,256,0,stream>>>(hn,w1,G);
  k_g2<<<S*D/256,256,0,stream>>>(G,w2,h,(u16*)d_out);
}
```

```cpp
#include <hip/hip_runtime.h>
#include <stdint.h>

#define LAS __attribute__((address_space(3)))
#define GAS __attribute__((address_space(1)))
typedef unsigned v4u __attribute__((ext_vector_type(4)));
typedef unsigned v2u __attribute__((ext_vector_type(2)));
typedef float f4 __attribute__((ext_vector_type(4)));
typedef float f2 __attribute__((ext_vector_type(2)));
typedef short s8 __attribute__((ext_vector_type(8)));
typedef short s4 __attribute__((ext_vector_type(4)));
typedef __bf16 bf2 __attribute__((ext_vector_type(2)));
typedef unsigned long long u64;
#define RLX_AGENT __ATOMIC_RELAXED, __HIP_MEMORY_SCOPE_AGENT
#define SC1 16

constexpr int S_ = 2048, D_ = 512, F_ = 1792;
constexpr int WS_EPOCH = 0;
constexpr int WS_FLAGA = 4096;
constexpr int WS_FLAGB = 16384;
constexpr int WS_FLAGC = 28672;
constexpr int WS_STATS = 32768;
constexpr int WS_XT = 65536;
constexpr int WS_QT = WS_XT + (2 << 20);
constexpr int WS_HP = WS_QT + (2 << 20);
constexpr int WS_G = WS_HP + (2 << 20);
constexpr int WS_END = WS_G + 128 * 56 * 1024;
constexpr int WS_STAMP = WS_END;
constexpr int L_W1 = 0;
constexpr int L_HK = 114688;
constexpr int HKS = 4160;
constexpr int L_XS = L_HK + 8 * HKS;
constexpr int XSS = 4608;
constexpr int L_CS = L_XS + 2 * XSS;
constexpr int L_CT = L_CS + 512;
constexpr int L_END = L_CT + 64;
static_assert(L_END <= 163840, "lds");
constexpr unsigned SPIN_CAP = 1u << 18;

__device__ __forceinline__ float lo_f(unsigned u) { return __uint_as_float(u << 16); }
__device__ __forceinline__ float hi_f(unsigned u) { return __uint_as_float(u & 0xffff0000u); }
__device__ __forceinline__ unsigned pk(float a, float b) { bf2 y; y.x = (__bf16)a; y.y = (__bf16)b; return __builtin_bit_cast(unsigned, y); }
__device__ __forceinline__ float rbf(float a) { return lo_f(pk(a, a)); }
__device__ __forceinline__ f4 mfma(v4u a, v4u b, f4 c) {
  return __builtin_amdgcn_mfma_f32_16x16x32_bf16(__builtin_bit_cast(__attribute__((ext_vector_type(8))) __bf16, a), __builtin_bit_cast(__attribute__((ext_vector_type(8))) __bf16, b), c, 0, 0, 0);
}
__device__ __forceinline__ float dot2(unsigned a, unsigned b, float c) {
  return __builtin_amdgcn_fdot2_f32_bf16(__builtin_bit_cast(bf2, a), __builtin_bit_cast(bf2, b), c, false);
}
template <int CTRL> __device__ __forceinline__ float dpp_add(float v) {
  return v + __uint_as_float(__builtin_amdgcn_update_dpp(0u, __float_as_uint(v), CTRL, 0xf, 0xf, true));
}
__device__ __forceinline__ float sum_rows(float v) {
  auto r = __builtin_amdgcn_permlane32_swap(__float_as_uint(v), __float_as_uint(v), false, false);
  v = __uint_as_float(r[0]) + __uint_as_float(r[1]);
  auto s = __builtin_amdgcn_permlane16_swap(__float_as_uint(v), __float_as_uint(v), false, false);
  return __uint_as_float(s[0]) + __uint_as_float(s[1]);
}
__device__ __forceinline__ float wave_sum(float v) {
  v = dpp_add<0xB1>(v); v = dpp_add<0x4E>(v); v = dpp_add<0x141>(v); v = dpp_add<0x140>(v);
  return sum_rows(v);
}
__device__ __forceinline__ void ln_fin(float s, float ss, float& m, float& den) {
  const float mean = s * (1.0f / D_);
  const float var = fmaxf(ss * (1.0f / D_) - mean * mean, 0.0f);
  m = rbf(mean);
  den = rbf(__builtin_sqrtf(rbf(rbf(var) + 1e-5f)));
}
#define DRAIN() asm volatile("s_waitcnt vmcnt(0)" ::: "memory")
#define LGKM0() asm volatile("s_waitcnt lgkmcnt(0)" ::: "memory")
#define BARRIER() do { asm volatile("s_waitcnt lgkmcnt(0)" ::: "memory"); __builtin_amdgcn_s_barrier(); asm volatile("" ::: "memory"); } while (0)
#define SB0() __builtin_amdgcn_sched_barrier(0)

#define STAMP(i)

__device__ __forceinline__ void poll256(__amdgpu_buffer_rsrc_t rs, int off, unsigned tag, int lane) {
  for (unsigned spins = 0; spins < SPIN_CAP; ++spins) {
    v4u f = __builtin_amdgcn_raw_buffer_load_b128(rs, off + lane * 16, 0, SC1);
    const bool ok = (f.x == tag) & (f.y == tag) & (f.z == tag) & (f.w == tag);
    if (__all(ok)) break;
    __builtin_amdgcn_s_sleep(1);
    asm volatile("" ::: "memory");
  }
  __builtin_amdgcn_fence(__ATOMIC_ACQUIRE, "wavefront");
  asm volatile("" ::: "memory");
}

template <int GN>
__device__ __forceinline__ void conv_loop(LAS const char* abase, LAS const char* bbase, f4 (&acc)[4]) {
  constexpr int NT[4] = {GN ? 6 : 7, GN ? 5 : 4, GN ? 2 : 3, GN ? 1 : 0};
  constexpr int NM = NT[0] + 1;
#pragma unroll 1
  for (int sp = 0; sp < 4; ++sp) {
    v4u B[NM];
#pragma unroll
    for (int i = 0; i < NM; ++i) B[i] = *(LAS const v4u*)(bbase + 512 * (7 - i));
#pragma unroll
    for (int m = 0; m < NM; ++m) {
      v2u a0, a1;
      asm volatile("ds_read_b64 %0, %2 offset:%3\n\tds_read_b64 %1, %2 offset:%4" : "=&v"(a0), "=&v"(a1) : "v"((unsigned)(size_t)abase), "i"(512 * m), "i"(512 * m + 8) : "memory");
      asm volatile("s_waitcnt lgkmcnt(0)" : "+v"(a0), "+v"(a1) :: "memory");
      v4u A = {a0.x, a0.y, a1.x, a1.y};
#pragma unroll
      for (int t = 0; t < 4; ++t)
        if (NT[t] - m >= 0) acc[t] = mfma(A, B[NT[t] - m], acc[t]);
    }
    abase += 64; bbase += 64;
  }
}

__global__ void __launch_bounds__(512, 2)
fused_kernel(const unsigned* __restrict__ gq, const unsigned* __restrict__ gk, const unsigned* __restrict__ gv,
             const unsigned* __restrict__ gsk, const unsigned* __restrict__ gfil, const unsigned* __restrict__ gD,
             const char* __restrict__ gw1, const char* __restrict__ gw2, char* __restrict__ gout, char* ws) {
  extern __shared__ __attribute__((aligned(16))) char smem_[];
  LAS char* L = (LAS char*)smem_;
  const int b = blockIdx.x, tid = threadIdx.x, lane = tid & 63;
  const int w = __builtin_amdgcn_readfirstlane(tid >> 6);
  const int xg = b & 7, idx = b >> 3;
  const __amdgpu_buffer_rsrc_t rs = __builtin_amdgcn_make_buffer_rsrc(ws, 0, WS_END, 0x00020000);
  STAMP(0);
  if (tid < 4) ((LAS unsigned*)(L + L_CT))[tid] = 0u;

  const int pair = xg * 32 + idx;
  const int pm = 2 * xg + (idx >> 4), cn = idx & 15;
  const int rt = 4 * xg + (idx >> 3), ct = idx & 7;
  const int kr = w ^ (((idx >> 3) & 1) << 2);
  const int l15 = lane & 15, lq = lane >> 4;

  v2u w2r[7][8];
  auto issue_w2 = [&]() {
    const char* base = gw2 + (size_t)(224 * kr + 4 * lq) * 1024 + ct * 128 + l15 * 8;
#pragma unroll
    for (int kk = 0; kk < 7; ++kk)
#pragma unroll
      for (int j = 0; j < 8; ++j) {
        const int fb = (kk < 3 ? 32 * kk + 16 * (j >> 2) : kk < 6 ? 112 + 32 * (kk - 3) + 16 * (j >> 2) : 96 + 112 * (j >> 2)) + (j & 3);
        w2r[kk][j] = *(const v2u*)(base + fb * 1024);
      }
    SB0();
  };

  v4u qv[2], st[2];
  if (w < 4) {
    const int R0 = idx * 64 + (w >> 1) * 32 + lq * 8;
    const int pc = xg * 32 + (w & 1) * 16 + l15;
    unsigned kd[11], vd[8], qd[8];
#pragma unroll
    for (int i = 0; i < 11; ++i) { const int r = R0 - 3 + i; kd[i] = gk[(r < 0 ? 0 : r) * 256 + pc]; }
#pragma unroll
    for (int i = 0; i < 8; ++i) vd[i] = gv[(R0 + i) * 256 + pc];
    const v4u skd = *(const v4u*)(gsk + pc * 4);
#pragma unroll
    for (int i = 0; i < 8; ++i) qd[i] = gq[(R0 + i) * 256 + pc];
    const v4u sr0 = *(const v4u*)(gq + (8 * b + 2 * w) * 256 + lane * 4);
    const v4u sr1 = *(const v4u*)(gq + (8 * b + 2 * w + 1) * 256 + lane * 4);
    const unsigned* fsrc = gfil + (2 * pair + (w >> 1)) * 1024;
    v4u fd[4]; unsigned fp[4];
#pragma unroll
    for (int n = 0; n < 4; ++n) { const int i = n * 64 + lane; fd[n] = *(const v4u*)(fsrc + 4 * i); fp[n] = fsrc[i ? 4 * i - 1 : 0]; }
    unsigned ep = __hip_atomic_load((GAS unsigned*)(ws + WS_EPOCH), RLX_AGENT);
    SB0();
    BARRIER();
    if (tid < 60) { *(LAS v4u*)(L + L_XS + (tid >= 30 ? XSS - 480 : 0) + 4096 + tid * 16) = v4u{0u, 0u, 0u, 0u}; }
    if (R0 == 0) { kd[0] = 0u; kd[1] = 0u; kd[2] = 0u; }
    const float a0 = lo_f(skd.x), a1 = hi_f(skd.x), a2 = lo_f(skd.y), a3 = hi_f(skd.y);
    const float b0 = lo_f(skd.z), b1 = hi_f(skd.z), b2 = lo_f(skd.w), b3 = hi_f(skd.w);
    float x0[8], x1[8];
#pragma unroll
    for (int i = 0; i < 8; ++i) {
      float s0 = lo_f(kd[i]) * a3; s0 = fmaf(lo_f(kd[i + 1]), a2, s0); s0 = fmaf(lo_f(kd[i + 2]), a1, s0); s0 = fmaf(lo_f(kd[i + 3]), a0, s0);
      float s1 = hi_f(kd[i]) * b3; s1 = fmaf(hi_f(kd[i + 1]), b2, s1); s1 = fmaf(hi_f(kd[i + 2]), b1, s1); s1 = fmaf(hi_f(kd[i + 3]), b0, s1);
      x0[i] = s0 * lo_f(vd[i]); x1[i] = s1 * hi_f(vd[i]);
    }
    STAMP(1);
    LAS char* tb = L + L_HK + 2 * w * HKS;
    {
      const v4u c0 = {pk(x0[7], x0[6]), pk(x0[5], x0[4]), pk(x0[3], x0[2]), pk(x0[1], x0[0])};
      const v4u c1 = {pk(x1[7], x1[6]), pk(x1[5], x1[4]), pk(x1[3], x1[2]), pk(x1[1], x1[0])};
      *(LAS v4u*)(tb + (2 * l15) * 80 + (3 - lq) * 16) = c0;
      *(LAS v4u*)(tb + (2 * l15 + 1) * 80 + (3 - lq) * 16) = c1;
      *(LAS v4u*)(tb + 4096 + l15 * 144 + lq * 32) = v4u{qd[0], qd[1], qd[2], qd[3]};
      *(LAS v4u*)(tb + 4096 + l15 * 144 + lq * 32 + 16) = v4u{qd[4], qd[5], qd[6], qd[7]};
      LGKM0();
      const int Rw = idx * 64 + (w >> 1) * 32;
      const int ch0 = xg * 64 + (w & 1) * 32;
#pragma unroll
      for (int n = 0; n < 2; ++n) {
        const int ch = n * 16 + (lane >> 2), cx = lane & 3;
        const v4u o = *(LAS const v4u*)(tb + ch * 80 + cx * 16);
        __builtin_amdgcn_raw_buffer_store_b128(o, rs, WS_XT + (ch0 + ch) * 4096 + (2016 - Rw) * 2 + cx * 16, 0, SC1);
      }
#pragma unroll
      for (int n = 0; n < 2; ++n) {
        const int pp = n * 8 + (lane >> 3), cx = lane & 7;
        const v4u o = *(LAS const v4u*)(tb + 4096 + pp * 144 + cx * 16);
        __builtin_amdgcn_raw_buffer_store_b128(o, rs, WS_QT + (ch0 / 2 + pp) * 8192 + Rw * 4 + cx * 16, 0, SC1);
      }
    }
    {
      const unsigned one = 0x3f803f80u;
      float s0 = dot2(sr0.x, one, 0.f), q0 = dot2(sr0.x, sr0.x, 0.f), s1 = dot2(sr1.x, one, 0.f), q1 = dot2(sr1.x, sr1.x, 0.f);
      s0 = dot2(sr0.y, one, s0); q0 = dot2(sr0.y, sr0.y, q0); s1 = dot2(sr1.y, one, s1); q1 = dot2(sr1.y, sr1.y, q1);
      s0 = dot2(sr0.z, one, s0); q0 = dot2(sr0.z, sr0.z, q0); s1 = dot2(sr1.z, one, s1); q1 = dot2(sr1.z, sr1.z, q1);
      s0 = dot2(sr0.w, one, s0); q0 = dot2(sr0.w, sr0.w, q0); s1 = dot2(sr1.w, one, s1); q1 = dot2(sr1.w, sr1.w, q1);
      s0 = wave_sum(s0); q0 = wave_sum(q0); s1 = wave_sum(s1); q1 = wave_sum(q1);
      float m0, d0, m1, d1;
      ln_fin(s0, q0, m0, d0); ln_fin(s1, q1, m1, d1);
      if (lane == 0) __builtin_amdgcn_raw_buffer_store_b64(v2u{pk(m0, d0), pk(m1, d1)}, rs, WS_STATS + (8 * b + 2 * w) * 4, 0, SC1);
    }
    STAMP(2);
    {
      LGKM0();
      LAS unsigned* ev = (LAS unsigned*)tb; LAS unsigned* od = (LAS unsigned*)(tb + HKS);
      const int sh = 8 - (w & 1);
      if (lane < 8) { ev[lane] = 0u; od[lane] = 0u; }
      if (lane >= 16 && lane < 28) { ev[1012 + lane] = 0u; od[1012 + lane] = 0u; }
      if (lane == 0) fp[0] = 0u;
#pragma unroll
      for (int n = 0; n < 4; ++n) {
        const int u = 4 * (n * 64 + lane) + sh;
        ev[u] = fd[n].x; ev[u + 1] = fd[n].y; ev[u + 2] = fd[n].z; ev[u + 3] = fd[n].w;
        od[u - 1] = __builtin_amdgcn_alignbit(fd[n].x, fp[n], 16);
        od[u] = __builtin_amdgcn_alignbit(fd[n].y, fd[n].x, 16);
        od[u + 1] = __builtin_amdgcn_alignbit(fd[n].z, fd[n].y, 16);
        od[u + 2] = __builtin_amdgcn_alignbit(fd[n].w, fd[n].z, 16);
      }
      if (lane == 63) od[1023 + sh] = fd[3].w >> 16;
    }
    DRAIN();
    STAMP(3);
    const unsigned tag = __builtin_amdgcn_readfirstlane(ep) + 1u;
    if (tid == 0) ((LAS unsigned*)(L + L_CT))[4] = tag;
    unsigned old = 0;
    if (lane == 0) old = __hip_atomic_fetch_add((LAS unsigned*)(L + L_CT), 1u, __ATOMIC_RELAXED, __HIP_MEMORY_SCOPE_WORKGROUP);
    old = __builtin_amdgcn_readfirstlane(old);
    if (old == 3u && lane < 8) __hip_atomic_store((GAS unsigned*)(ws + WS_FLAGA + lane * 1024) + b, tag, RLX_AGENT);
    poll256(rs, WS_FLAGA + xg * 1024, tag, lane);
    STAMP(4);
    const v4u xa = __builtin_amdgcn_raw_buffer_load_b128(rs, WS_XT + pair * 8192 + tid * 16, 0, SC1);
    const v4u xb = __builtin_amdgcn_raw_buffer_load_b128(rs, WS_XT + pair * 8192 + 4096 + tid * 16, 0, SC1);
    SB0();
    issue_w2();
    *(LAS v4u*)(L + L_XS + tid * 16) = xa;
    *(LAS v4u*)(L + L_XS + XSS + tid * 16) = xb;
    STAMP(5);
    BARRIER();
  } else {
    const int wb = w - 4;
    BARRIER();
    const char* src0 = gw1 + cn * 224 + (size_t)wb * 128 * 3584;
#pragma unroll
    for (int p = 0; p < 7; ++p) {
      const int u = p * 64 + lane, row = (u * 4682) >> 16, cc = u - row * 14;
      const char* src = src0 + row * 3584 + cc * 16;
#pragma unroll
      for (int g = 0; g < 4; ++g) {
        unsigned keep;
        const unsigned dst = (unsigned)(size_t)(L + L_W1) + (wb * 28 + g * 7 + p) * 1024;
        asm volatile("s_mov_b32 %0, m0\n\ts_mov_b32 m0, %2\n\ts_nop 0\n\tglobal_load_lds_dwordx4 %1, off\n\ts_mov_b32 m0, %0" : "=&s"(keep) : "v"(src + g * 32 * 3584), "s"(dst) : "memory");
      }
    }
    issue_w2();
    STAMP(5);
    BARRIER();
    const int u = tid - 256;
    qv[0] = __builtin_amdgcn_raw_buffer_load_b128(rs, WS_QT + pair * 8192 + u * 16, 0, SC1);
    qv[1] = __builtin_amdgcn_raw_buffer_load_b128(rs, WS_QT + pair * 8192 + 4096 + u * 16, 0, SC1);
    st[0] = __builtin_amdgcn_raw_buffer_load_b128(rs, WS_STATS + u * 16, 0, SC1);
    st[1] = __builtin_amdgcn_raw_buffer_load_b128(rs, WS_STATS + 4096 + u * 16, 0, SC1);
    SB0();
  }
  STAMP(6);
  const unsigned tag = __builtin_amdgcn_readfirstlane(((LAS unsigned*)(L + L_CT))[4]);
  {
    const int ch = w >> 2, hs = (w >> 1) & 1, gn = w & 1;
    LAS const char* abase = L + L_HK + (ch * 4 + ((l15 + 1) & 3)) * HKS + 2 * (((l15 + 1) & ~3) + 8 * lq + 128 * hs);
    LAS const char* bbase = L + L_XS + ch * XSS + 2 * (2032 - 256 * 7 - 16 * l15 + 8 * lq + 128 * hs);
    f4 acc[4];
#pragma unroll
    for (int t = 0; t < 4; ++t) acc[t] = f4{0.f, 0.f, 0.f, 0.f};
    if (gn) conv_loop<1>(abase, bbase, acc); else conv_loop<0>(abase, bbase, acc);
    STAMP(7);
    if (w >= 4) DRAIN();
    BARRIER();
    LAS char* P = L + L_HK + (ch * 2 + hs) * 8192 + l15 * 64 + lq * 16;
    *(LAS f4*)(P + (gn ? 6 : 7) * 1024) = acc[0];
    *(LAS f4*)(P + (gn ? 5 : 4) * 1024) = acc[1];
    *(LAS f4*)(P + (gn ? 2 : 3) * 1024) = acc[2];
    *(LAS f4*)(P + (gn ? 1 : 0) * 1024) = acc[3];
    BARRIER();
  }
  v4u w2f[7][4];
  auto perm_w2 = [&]() {
#pragma unroll
    for (int kk = 0; kk < 7; ++kk)
#pragma unroll
      for (int t = 0; t < 4; ++t)
#pragma unroll
        for (int i = 0; i < 4; ++i)
          w2f[kk][t][i] = __builtin_amdgcn_perm(w2r[kk][2 * i + 1][t >> 1], w2r[kk][2 * i][t >> 1], (t & 1) ? 0x07060302u : 0x05040100u);
    SB0();
  };
  LAS const char* wbase = L + L_W1 + (4 * lq + (l15 >> 2)) * 224 + (l15 & 3) * 8;
  auto w1frag = [&](int ks, int ft) -> v4u {
    const s4 lo = __builtin_amdgcn_ds_read_tr16_b64_v4i16((LAS s4*)(wbase + ks * 32 * 224 + ft * 32));
    const s4 hi = __builtin_amdgcn_ds_read_tr16_b64_v4i16((LAS s4*)(wbase + ks * 32 * 224 + 16 * 224 + ft * 32));
    const v2u a = __builtin_bit_cast(v2u, lo), c = __builtin_bit_cast(v2u, hi);
    return v4u{a.x, a.y, c.x, c.y};
  };
  if (w >= 4) {
    const int u = tid - 256;
    const unsigned dd = gD[pair];
    const float D0 = lo_f(dd), D1 = hi_f(dd);
    const int r16 = pair & 15;
    const int slot = (pair & ~15) + 4 * (2 * (r16 >> 3) + (r16 & 1)) + ((r16 >> 1) & 3);
#pragma unroll
    for (int hh = 0; hh < 2; ++hh) {
      const int t0 = hh * 1024 + 4 * u;
      const f4 y0 = *(LAS const f4*)(L + L_HK + t0 * 4) + *(LAS const f4*)(L + L_HK + 8192 + t0 * 4);
      const f4 y1 = *(LAS const f4*)(L + L_HK + 16384 + t0 * 4) + *(LAS const f4*)(L + L_HK + 24576 + t0 * 4);
      const v2u k0 = *(LAS const v2u*)(L + L_XS + (2044 - t0) * 2);
      const v2u k1 = *(LAS const v2u*)(L + L_XS + XSS + (2044 - t0) * 2);
      const float kv0[4] = {hi_f(k0.y), lo_f(k0.y), hi_f(k0.x), lo_f(k0.x)};
      const float kv1[4] = {hi_f(k1.y), lo_f(k1.y), hi_f(k1.x), lo_f(k1.x)};
      v4u o;
#pragma unroll
      for (int e = 0; e < 4; ++e) {
        const float m = lo_f(st[hh][e]), r = __builtin_amdgcn_rcpf(hi_f(st[hh][e]));
        const float q0 = lo_f(qv[hh][e]), q1 = hi_f(qv[hh][e]);
        const float h0 = fmaf((q0 - m) * r, fmaf(kv0[e], D0, y0[e]), q0);
        const float h1 = fmaf((q1 - m) * r, fmaf(kv1[e], D1, y1[e]), q1);
        o[e] = pk(h0, h1);
      }
      __builtin_amdgcn_raw_buffer_store_b128(o, rs, WS_HP + (t0 >> 4) * 16384 + slot * 64 + (t0 & 15) * 4, 0, SC1);
    }
    STAMP(8);
    DRAIN();
    unsigned old = 0;
    if (lane == 0) old = __hip_atomic_fetch_add((LAS unsigned*)(L + L_CT + 4), 1u, __ATOMIC_RELAXED, __HIP_MEMORY_SCOPE_WORKGROUP);
    old = __builtin_amdgcn_readfirstlane(old);
    if (old == 3u && lane < 8) __hip_atomic_store((GAS unsigned*)(ws + WS_FLAGB + lane * 1024) + b, tag, RLX_AGENT);
    STAMP(9);
    perm_w2();
    if (w == 4) poll256(rs, WS_FLAGB + xg * 1024, tag, lane);
  } else {
    const v4u ones = {0x3f803f80u, 0x3f803f80u, 0x3f803f80u, 0x3f803f80u};
    for (int ft = w; ft < 7; ft += 4) {
      f4 c = {0.f, 0.f, 0.f, 0.f};
#pragma unroll 4
      for (int ks = 0; ks < 16; ++ks) c = mfma(w1frag(ks, ft), ones, c);
      if (l15 == 0) *(LAS f4*)(L + L_CS + (ft * 16 + lq * 4) * 4) = c;
    }
    perm_w2();
  }
  BARRIER();
  STAMP(10);
  if (b == 0 && tid == 0) __hip_atomic_store((GAS unsigned*)(ws + WS_EPOCH), tag, RLX_AGENT);
  unsigned hres[4];
  {
    const int stile = 8 * pm + w;
    v4u hb[16];
#pragma unroll
    for (int ks = 0; ks < 16; ++ks)
#pragma unroll
      for (int i = 0; i < 4; ++i)
        hb[ks][i] = __builtin_amdgcn_raw_buffer_load_b32(rs, WS_HP + stile * 16384 + ks * 1024 + i * 256 + lane * 4, 0, SC1);
    SB0();
    f4 acc[7];
#pragma unroll
    for (int t = 0; t < 7; ++t) acc[t] = f4{0.f, 0.f, 0.f, 0.f};
    float sm = 0.f, sq = 0.f;
#pragma unroll
    for (int ks = 0; ks < 16; ++ks) {
#pragma unroll
      for (int t = 0; t < 7; ++t) acc[t] = mfma(w1frag(ks, t), hb[ks], acc[t]);
#pragma unroll
      for (int i = 0; i < 4; ++i) { sm = dot2(hb[ks][i], 0x3f803f80u, sm); sq = dot2(hb[ks][i], hb[ks][i], sq); }
    }
    STAMP(11);
    {
      const int row = tid >> 3, oc = tid & 7;
#pragma unroll
      for (int i = 0; i < 4; ++i) {
        const int p = ct * 32 + oc * 4 + i, r16 = p & 15;
        const int slot = (p & ~15) + 4 * (2 * (r16 >> 3) + (r16 & 1)) + ((r16 >> 1) & 3);
        hres[i] = __builtin_amdgcn_raw_buffer_load_b32(rs, WS_HP + (4 * rt + (row >> 4)) * 16384 + slot * 64 + (row & 15) * 4, 0, SC1);
      }
    }
    sm = sum_rows(sm); sq = sum_rows(sq);
    float m, den;
    ln_fin(sm, sq, m, den);
    const float rs_ = __builtin_amdgcn_rcpf(den);
    v2u g[7];
#pragma unroll
    for (int t = 0; t < 7; ++t) {
      const f4 c = *(LAS const f4*)(L + L_CS + (t * 16 + lq * 4) * 4);
      float o[4];
#pragma unroll
      for (int e = 0; e < 4; ++e) {
        const float x = (acc[t][e] - m * c[e]) * rs_;
        o[e] = x * __builtin_amdgcn_rcpf(1.0f + __expf(-x));
      }
      g[t] = v2u{pk(o[0], o[1]), pk(o[2], o[3])};
    }
    const int gb = WS_G + (stile * 56 + (cn >> 1) * 7) * 1024 + lane * 16;
#pragma unroll
    for (int kk = 0; kk < 3; ++kk)
      __builtin_amdgcn_raw_buffer_store_b128(v4u{g[2 * kk].x, g[2 * kk].y, g[2 * kk + 1].x, g[2 * kk + 1].y}, rs, gb + (3 * (cn & 1) + kk) * 1024, 0, SC1);
    __builtin_amdgcn_raw_buffer_store_b64(g[6], rs, gb + 6 * 1024 + (cn & 1) * 8, 0, SC1);
    STAMP(12);
    DRAIN();
    BARRIER();
    if (tid == 0) __hip_atomic_store((GAS unsigned*)(ws + WS_FLAGC + pm * 256) + cn, tag, RLX_AGENT);
  }
  STAMP(13);
  {
    GAS u64* fl = (GAS u64*)(ws + WS_FLAGC + (rt >> 1) * 256) + kr;
    const u64 want = ((u64)tag << 32) | tag;
    for (unsigned spins = 0; spins < SPIN_CAP; ++spins) {
      const u64 f = __hip_atomic_load(fl, RLX_AGENT);
      if (__all(f == want)) break;
      __builtin_amdgcn_s_sleep(1);
    }
    __builtin_amdgcn_fence(__ATOMIC_ACQUIRE, "wavefront");
    asm volatile("" ::: "memory");
    STAMP(14);
    v4u gf[4][7];
#pragma unroll
    for (int sg = 0; sg < 4; ++sg)
#pragma unroll
      for (int kk = 0; kk < 7; ++kk)
        gf[sg][kk] = __builtin_amdgcn_raw_buffer_load_b128(rs, WS_G + ((4 * rt + sg) * 56 + kr * 7 + kk) * 1024 + lane * 16, 0, SC1);
    SB0();
#pragma unroll
    for (int sg = 0; sg < 4; ++sg) {
      f4 acc[4];
#pragma unroll
      for (int t = 0; t < 4; ++t) acc[t] = f4{0.f, 0.f, 0.f, 0.f};
#pragma unroll
      for (int kk = 0; kk < 7; ++kk)
#pragma unroll
        for (int t = 0; t < 4; ++t) acc[t] = mfma(w2f[kk][t], gf[sg][kk], acc[t]);
#pragma unroll
      for (int t = 0; t < 4; ++t) *(LAS f4*)(L + ((w * 4 + sg) * 4 + t) * 1280 + lq * 320 + l15 * 16) = acc[t];
    }
    BARRIER();
    const int row = tid >> 3, oc = tid & 7;
    LAS const char* pr = L + ((row >> 4) * 4) * 1280 + (oc >> 1) * 320 + (row & 15) * 16 + (oc & 1) * 8;
    f2 s[4];
#pragma unroll
    for (int t = 0; t < 4; ++t) s[t] = *(LAS const f2*)(pr + t * 1280);
#pragma unroll
    for (int ww = 1; ww < 8; ++ww)
#pragma unroll
      for (int t = 0; t < 4; ++t) s[t] += *(LAS const f2*)(pr + (ww * 16 + t) * 1280);
    v4u o;
    o.x = pk(lo_f(hres[0]) + rbf(s[0].x), hi_f(hres[0]) + rbf(s[1].x));
    o.y = pk(lo_f(hres[1]) + rbf(s[2].x), hi_f(hres[1]) + rbf(s[3].x));
    o.z = pk(lo_f(hres[2]) + rbf(s[0].y), hi_f(hres[2]) + rbf(s[1].y));
    o.w = pk(lo_f(hres[3]) + rbf(s[2].y), hi_f(hres[3]) + rbf(s[3].y));
    *(v4u*)(gout + (size_t)(rt * 64 + row) * 1024 + ct * 128 + oc * 16) = o;
  }
  STAMP(15);
}

extern "C" void kernel_launch(void* const* d_in, const int* in_sizes, int n_in, void* d_out, int out_size, void* d_ws, size_t ws_size, hipStream_t stream) {
  (void)hipFuncSetAttribute((const void*)fused_kernel, hipFuncAttributeMaxDynamicSharedMemorySize, 163840);
  fused_kernel<<<256, 512, 163840, stream>>>((const unsigned*)d_in[0], (const unsigned*)d_in[1], (const unsigned*)d_in[2],
      (const unsigned*)d_in[3], (const unsigned*)d_in[4], (const unsigned*)d_in[5], (const char*)d_in[7], (const char*)d_in[8],
      (char*)d_out, (char*)d_ws);
}
```

```cpp
#include <hip/hip_runtime.h>
#include <stdint.h>
#include <type_traits>

#ifdef __HIP_DEVICE_COMPILE__
#pragma clang attribute push(__attribute__((target("no-packed-fp32-ops"))), apply_to = function)
#endif
#define LAS __attribute__((address_space(3)))
#define GAS __attribute__((address_space(1)))
typedef unsigned v4u __attribute__((ext_vector_type(4)));
typedef unsigned v2u __attribute__((ext_vector_type(2)));
typedef float f4 __attribute__((ext_vector_type(4)));
typedef float f2 __attribute__((ext_vector_type(2)));
typedef short s4 __attribute__((ext_vector_type(4)));
typedef __bf16 bf2 __attribute__((ext_vector_type(2)));
typedef __bf16 bf8 __attribute__((ext_vector_type(8)));
typedef unsigned long long u64;
#define RLX_AGENT __ATOMIC_RELAXED, __HIP_MEMORY_SCOPE_AGENT
#define RLX_WG __ATOMIC_RELAXED, __HIP_MEMORY_SCOPE_WORKGROUP
#define SC1 16

constexpr int D_ = 512;
constexpr int WS_EPOCH = 0;
constexpr int WS_FA1 = 4096;
constexpr int WS_FC = 20480;
constexpr int WS_STATS = 36864;
constexpr int WS_FA2 = 65536;
constexpr int WS_FB = 98304;
constexpr int WS_XT = 262144;
constexpr int WS_QT = WS_XT + (2 << 20);
constexpr int WS_HP = WS_QT + (2 << 20);
constexpr int WS_G = WS_HP + (2 << 20);
constexpr int WS_W2 = WS_G + 128 * 56 * 1024;
constexpr int WS_END = WS_W2 + 1792 * 1024;
constexpr int WS_STAMP = WS_END;
constexpr int L_W1 = 0;
constexpr int L_HK = 114688;
constexpr int HKS = 4160;
constexpr int L_XS = L_HK + 8 * HKS;
constexpr int XSS = 4608;
constexpr int L_CS = L_XS + 10240;
constexpr int L_CT = L_CS + 512;
constexpr int L_END = L_CT + 128;
static_assert(L_END <= 163840, "lds");
enum { C_A1 = 0, C_A2, C_BE, C_BL, C_CE, C_CL, R_BE, R_BL, R_CE, C_CS, C_W1, C_XS, C_HK, F_A, R_A1, R_A2, C_N };
constexpr unsigned SPIN_CAP = 1u << 18;

__device__ __forceinline__ float u2f(unsigned u) { return __builtin_bit_cast(float, u); }
__device__ __forceinline__ unsigned f2u(float f) { return __builtin_bit_cast(unsigned, f); }
__device__ __forceinline__ float lo_f(unsigned u) { return u2f(u << 16); }
__device__ __forceinline__ float hi_f(unsigned u) { return u2f(u & 0xffff0000u); }
__device__ __forceinline__ unsigned pk(float a, float b) { bf2 y; y.x = (__bf16)a; y.y = (__bf16)b; return __builtin_bit_cast(unsigned, y); }
__device__ __forceinline__ float rbf(float a) { return lo_f(pk(a, a)); }
__device__ __forceinline__ f4 mfma(v4u a, v4u b, f4 c) {
  return __builtin_amdgcn_mfma_f32_16x16x32_bf16(__builtin_bit_cast(bf8, a), __builtin_bit_cast(bf8, b), c, 0, 0, 0);
}
__device__ __forceinline__ float dot2(unsigned a, unsigned b, float c) {
  return __builtin_amdgcn_fdot2_f32_bf16(__builtin_bit_cast(bf2, a), __builtin_bit_cast(bf2, b), c, false);
}
template <int CTRL> __device__ __forceinline__ float dpp_add(float v) {
  return v + u2f(__builtin_amdgcn_update_dpp(0u, f2u(v), CTRL, 0xf, 0xf, true));
}
__device__ __forceinline__ float sum_rows(float v) {
  auto r = __builtin_amdgcn_permlane32_swap(f2u(v), f2u(v), false, false);
  v = u2f(r[0]) + u2f(r[1]);
  auto s = __builtin_amdgcn_permlane16_swap(f2u(v), f2u(v), false, false);
  return u2f(s[0]) + u2f(s[1]);
}
__device__ __forceinline__ void ln_fin(float s, float ss, float& m, float& den) {
  const float mean = s * (1.0f / D_);
  const float var = __builtin_fmaxf(ss * (1.0f / D_) - mean * mean, 0.0f);
  m = rbf(mean);
  den = rbf(__builtin_sqrtf(rbf(rbf(var) + 1e-5f)));
}
#define DRAIN() asm volatile("s_waitcnt vmcnt(0)" ::: "memory")
#define LGKM0() asm volatile("s_waitcnt lgkmcnt(0)" ::: "memory")
#define BARRIER() do { __builtin_amdgcn_sched_barrier(0); asm volatile("s_waitcnt lgkmcnt(0)" ::: "memory"); __builtin_amdgcn_s_barrier(); asm volatile("" ::: "memory"); __builtin_amdgcn_sched_barrier(0); } while (0)
#define SB0() __builtin_amdgcn_sched_barrier(0)
#define FENCE() asm volatile("" ::: "memory")

#define STAMP(i)

#define PSLEEP 4
__device__ __forceinline__ void poll256(__amdgpu_buffer_rsrc_t rs, int off, unsigned tag, int lane) {
  const int o = off + lane * 16;
#pragma unroll 1
  for (unsigned spins = 0; spins < SPIN_CAP; ++spins) {
    const unsigned f0 = __builtin_amdgcn_raw_buffer_load_b32(rs, o, 0, SC1), f1 = __builtin_amdgcn_raw_buffer_load_b32(rs, o + 1024, 0, SC1);
    const unsigned f2 = __builtin_amdgcn_raw_buffer_load_b32(rs, o + 2048, 0, SC1), f3 = __builtin_amdgcn_raw_buffer_load_b32(rs, o + 3072, 0, SC1);
    const bool ok = (f0 == tag) & (f1 == tag) & (f2 == tag) & (f3 == tag);
    if (__builtin_amdgcn_ballot_w64(!ok) == 0) break;
    __builtin_amdgcn_s_sleep(PSLEEP);
    FENCE();
  }
  FENCE();
}
template <int N>
__device__ __forceinline__ void polln(__amdgpu_buffer_rsrc_t rs, int off, unsigned tag, int lane) {
  const int o = off + (lane & (N - 1)) * 32;
#pragma unroll 1
  for (unsigned spins = 0; spins < SPIN_CAP; ++spins) {
    const unsigned f = __builtin_amdgcn_raw_buffer_load_b32(rs, o, 0, SC1);
    if (__builtin_amdgcn_ballot_w64(f != tag) == 0) break;
    __builtin_amdgcn_s_sleep(PSLEEP);
    FENCE();
  }
  FENCE();
}
__device__ __forceinline__ void delay(int ticks) {
  if (ticks <= 0) return;
  const u64 t0 = __builtin_amdgcn_s_memrealtime();
  while (__builtin_amdgcn_s_memrealtime() - t0 < (u64)ticks) __builtin_amdgcn_s_sleep(1);
}
#define DA1 40
#define DBE 0
#define DBL 40
#define DCE 40
__device__ __forceinline__ bool arrive(LAS unsigned* c, unsigned last, int lane) {
  unsigned old = 0;
  if (lane == 0) old = __hip_atomic_fetch_add(c, 1u, RLX_WG);
  return (unsigned)__builtin_amdgcn_readfirstlane(old) == last;
}
__device__ __forceinline__ void lds_wait(LAS unsigned* c, unsigned v) {
#pragma unroll 1
  for (unsigned spins = 0; spins < (SPIN_CAP << 4); ++spins) {
    if ((unsigned)__builtin_amdgcn_readfirstlane(__hip_atomic_load(c, RLX_WG)) == v) break;
    __builtin_amdgcn_s_sleep(1);
  }
  FENCE();
}
__device__ __forceinline__ void glds16(const char* src, unsigned off, unsigned dst) {
  unsigned keep;
  asm volatile("s_mov_b32 %0, m0\n\ts_mov_b32 m0, %3\n\ts_nop 0\n\tglobal_load_lds_dwordx4 %2, %1\n\ts_mov_b32 m0, %0" : "=&s"(keep) : "s"(src), "v"(off), "s"(dst) : "memory");
}
__device__ __forceinline__ int hslot(int p) { const int r = p & 15; return (p & ~15) + 4 * (2 * (r >> 3) + (r & 1)) + ((r >> 1) & 3); }
__device__ __forceinline__ unsigned ab(unsigned hi, unsigned lo) { return __builtin_amdgcn_alignbit(hi, lo, 16); }

constexpr int cv_nt(int i) { int n = 0; while ((n + 1) * (n + 2) <= i) ++n; return n; }
#define W1E 6
#define NBL 8
#define NBE 12
#define CW0 6
#define ROT1 1
#define IC(x) std::integral_constant<int, x>{}
#define VMCNT(n) asm volatile("s_waitcnt vmcnt(%0)" :: "n"(n) : "memory")
#define STAMP_ARG
#define STAMP_PASS

template <int HALF>
__device__ __forceinline__ void rest(LAS char* L, const __amdgpu_buffer_rsrc_t rs, char* ws, char* __restrict__ gout, const int b, const int tid, const int w,
                                     const unsigned tag, const unsigned dd, const int rot1, v4u (&A)[8][2], const v4u qv, const v4u st,
                                     const char* w1src, const unsigned (&so)[7] STAMP_ARG) {
  LAS unsigned* CT = (LAS unsigned*)(L + L_CT);
  const int lane = tid & 63, l15 = lane & 15, lq = lane >> 4;
  const int xg = b & 7, idx = b >> 3;
  const int pair = xg * 32 + idx;
  const int pm = 4 * (xg >> 1) + (idx >> 3), cn = 8 * (xg & 1) + (idx & 7);
  const int rt = 4 * xg + (idx >> 3), ct = idx & 7;
  const f4 z4 = {0.f, 0.f, 0.f, 0.f};
  v4u w2f[28];
  const int w2o = WS_W2 + (ct * 8 + w) * 28 * 1024 + lane * 16;
  auto w2load = [&](int i) { w2f[i] = __builtin_amdgcn_raw_buffer_load_b128(rs, w2o + (i & 3) * 1024, (i >> 2) * 4096, SC1); };
  constexpr int NB = HALF ? NBL : NBE;
  {
    v4u B[8][2];
    LAS const char* bbase = L + L_XS + HALF * XSS + 2 * (2032 - 256 * 7 - 16 * l15 + 8 * lq + 64 * (w & 3));
#pragma unroll
    for (int i = 0; i < 4; ++i)
#pragma unroll
      for (int sp = 0; sp < 2; ++sp) B[i][sp] = *(LAS const v4u*)(bbase + 512 * (7 - i) + 64 * sp);
    SB0();
    f4 acc[8];
    LAS char* P = L + L_HK + w * 4096 + l15 * 64 + ((lq ^ (l15 >> 1)) & 3) * 16;
    auto seg = [&](auto I0, auto I1) {
#pragma unroll
      for (int i = I0; i < I1; ++i) {
        const int nt = cv_nt(i), r = i - nt * (nt + 1), m = r >> 1, sp = r & 1;
        acc[nt] = mfma(A[m][sp], B[nt - m][sp], r == 0 ? z4 : acc[nt]);
        if (r == 2 && nt != 5) *(LAS f4*)(P + ((nt - 1) & 3) * 1024) = acc[nt - 1];
        if (i >= 6 && i < 14) B[4 + ((i - 6) >> 1)][i & 1] = *(LAS const v4u*)(bbase + 512 * (3 - ((i - 6) >> 1)) + 64 * (i & 1));
        if ((i & 3) == 1) {
          const int j = i >> 2;
          if (HALF == 0) { if (j < 6 && j < NBE) w2load(j); else if (j >= 12 && j - 6 < NBE) w2load(j - 6); }
          else { if (j < 7) glds16(w1src, so[j], (unsigned)(size_t)(L + L_W1) + (8 + w) * 7168 + j * 1024); else if (j - 7 < NBL) w2load(j - 7); }
        }
        SB0();
      }
    };
    auto epi_load = [&](f4 (&y)[8], v2u& k0, v2u& k1) {
      LAS const char* p = L + L_HK + ((tid & 255) ^ ((tid >> 3) & 3)) * 16;
#pragma unroll
      for (int j = 0; j < 8; ++j) y[j] = *(LAS const f4*)(p + j * 4096);
      k0 = *(LAS const v2u*)(L + L_XS + (2044 - 4 * tid) * 2);
      k1 = *(LAS const v2u*)(L + L_XS + XSS + (2044 - 4 * tid) * 2);
    };
    auto epi = [&](f4 (&y)[8], v2u k0, v2u k1) {
      const float D0 = lo_f(dd), D1 = hi_f(dd);
      const float kv0[4] = {hi_f(k0.y), lo_f(k0.y), hi_f(k0.x), lo_f(k0.x)};
      const float kv1[4] = {hi_f(k1.y), lo_f(k1.y), hi_f(k1.x), lo_f(k1.x)};
      const f4 y0 = (y[0] + y[1]) + (y[2] + y[3]), y1 = (y[4] + y[5]) + (y[6] + y[7]);
      v4u o;
#pragma unroll
      for (int e = 0; e < 4; ++e) {
        const float m = lo_f(st[e]), r = __builtin_amdgcn_rcpf(hi_f(st[e]));
        const float q0 = lo_f(qv[e]), q1 = hi_f(qv[e]);
        const float h0 = __builtin_fmaf((q0 - m) * r, __builtin_fmaf(kv0[e], D0, y0[e]), q0);
        const float h1 = __builtin_fmaf((q1 - m) * r, __builtin_fmaf(kv1[e], D1, y1[e]), q1);
        o[e] = pk(h0, h1);
      }
      __builtin_amdgcn_raw_buffer_store_b128(o, rs, WS_HP + (tid >> 2) * 16384 + hslot(pair) * 64 + (tid & 3) * 16, 0, SC1);
      FENCE();
      SB0();
    };
    seg(IC(0), IC(23));
    BARRIER();
    STAMP(7);
    if (HALF == 0) {
      f4 y[8]; v2u k0, k1;
      epi_load(y, k0, k1);
      BARRIER();
      epi(y, k0, k1);
      STAMP(8);
      seg(IC(23), IC(33));
    } else {
      seg(IC(23), IC(33));
      BARRIER();
    }
    *(LAS f4*)(P) = acc[4];
    seg(IC(33), IC(48));
    if (HALF == 0) {
      DRAIN();
      if (arrive(CT + C_BE, 3, lane) && lane < 8) __hip_atomic_store((GAS unsigned*)(ws + WS_FB + lane * 4096 + b * 16), tag, RLX_AGENT);
      STAMP(9);
    }
    seg(IC(48), IC(72));
    *(LAS f4*)(P + 3 * 1024) = acc[7];
    if (HALF == 1) VMCNT(NBL);
    BARRIER();
    STAMP(10);
    if (HALF == 1) {
      f4 y[8]; v2u k0, k1;
      epi_load(y, k0, k1);
      epi(y, k0, k1);
      STAMP(8);
    }
  }
  LAS const char* wbase = L + L_W1 + (4 * lq + (l15 >> 2)) * 224 + (l15 & 3) * 8;
  auto w1frag = [&](LAS const char* wb_, int ks, int ft) -> v4u {
    const s4 lo = __builtin_amdgcn_ds_read_tr16_b64_v4i16((LAS s4*)(wb_ + ks * 32 * 224 + ft * 32));
    const s4 hi = __builtin_amdgcn_ds_read_tr16_b64_v4i16((LAS s4*)(wb_ + ks * 32 * 224 + 16 * 224 + ft * 32));
    const v2u a = __builtin_bit_cast(v2u, lo), c = __builtin_bit_cast(v2u, hi);
    return v4u{a.x, a.y, c.x, c.y};
  };
  const v4u ones = {0x3f803f80u, 0x3f803f80u, 0x3f803f80u, 0x3f803f80u};
  if (HALF == 1) {
    LAS const char* cb = wbase + (w - 4) * 32;
    LAS const char* cb2 = cb + (w == 7 ? 0 : 128);
    f4 c0 = z4, c1 = z4;
#pragma unroll 4
    for (int ks = 0; ks < 8; ++ks) { c0 = mfma(w1frag(cb, ks, 0), ones, c0); c1 = mfma(w1frag(cb2, ks, 0), ones, c1); }
    DRAIN();
    if (arrive(CT + C_BL, 3, lane) && lane < 8) __hip_atomic_store((GAS unsigned*)(ws + WS_FB + 32768 + lane * 4096 + b * 16), tag, RLX_AGENT);
    STAMP(9);
#pragma unroll 4
    for (int ks = 8; ks < 16; ++ks) { c0 = mfma(w1frag(cb, ks, 0), ones, c0); c1 = mfma(w1frag(cb2, ks, 0), ones, c1); }
    if (l15 == 0) { *(LAS f4*)(L + L_CS + ((w - 4) * 16 + lq * 4) * 4) = c0; *(LAS f4*)(L + L_CS + (w * 16 + lq * 4) * 4) = c1; }
    LGKM0();
    (void)arrive(CT + C_CS, 0, lane);
  }
  if ((w & 3) == 0) {
    delay(HALF ? DBL : DBE);
    poll256(rs, WS_FB + HALF * 32768 + xg * 4096, tag, lane);
    if (lane == 0) __hip_atomic_store(CT + R_BE + HALF, 1u, RLX_WG);
  } else {
    lds_wait(CT + R_BE + HALF, 1u);
  }
  STAMP(11);
  {
    const int stile = 64 * HALF + 4 * pm + (w & 3);
    v4u hb[16];
#pragma unroll
    for (int ks = 0; ks < 16; ++ks)
#pragma unroll
      for (int i = 0; i < 4; ++i)
        hb[ks][i] = __builtin_amdgcn_raw_buffer_load_b32(rs, WS_HP + stile * 16384 + i * 256 + lane * 4, ((ks + rot1) & 15) * 1024, SC1);
    LAS const char* wbase2 = wbase + 8 * 32 * 224;
    v4u wf[7];
#pragma unroll
    for (int t = 0; t < 7; ++t) wf[t] = w1frag(wbase, 0, t);
    SB0();
    f4 acc[7], asum, asq;
#pragma unroll
    for (int ks = 0; ks < 16; ++ks) {
      asum = mfma(ones, hb[ks], ks ? asum : z4);
      asq = mfma(hb[ks], hb[ks], ks ? asq : z4);
      SB0();
#pragma unroll
      for (int t = 0; t < 7; ++t) {
        acc[t] = mfma(wf[t], hb[ks], ks ? acc[t] : z4);
        if (ks < 15) wf[t] = ks + 1 < 8 ? w1frag(wbase, ks + 1, t) : w1frag(wbase2, ks + 1 - 8, t);
        if (ks >= CW0 && (t == 1 || t == 5) && NB + 2 * (ks - CW0) + (t >> 2) < 28) w2load(NB + 2 * (ks - CW0) + (t >> 2));
        SB0();
      }
    }
    static_assert(NB + 2 * (16 - CW0) >= 28, "w2");
    (void)arrive(CT + C_W1, 0, lane);
    STAMP(12);
    if (HALF == 1 && b == 0 && tid == 256) __hip_atomic_store((GAS unsigned*)(ws + WS_EPOCH), tag, RLX_AGENT);
    float dg = (l15 & 2) ? ((l15 & 1) ? asq[3] : asq[2]) : ((l15 & 1) ? asq[1] : asq[0]);
    dg = (lq == (l15 >> 2)) ? dg : 0.f;
    const float sq = sum_rows(dg);
    float m, den;
    ln_fin(asum[0], sq, m, den);
    const float nl = -1.4426950408889634f * __builtin_amdgcn_rcpf(den), mn = -m * nl;
    lds_wait(CT + C_CS, 4u);
    v2u g[7];
#pragma unroll
    for (int t = 0; t < 7; ++t) {
      const f4 c = *(LAS const f4*)(L + L_CS + (t * 16 + lq * 4) * 4);
      float o[4];
#pragma unroll
      for (int e = 0; e < 4; ++e) {
        const float z = __builtin_fmaf(acc[t][e], nl, c[e] * mn);
        o[e] = z * __builtin_amdgcn_rcpf(__builtin_fmaf(__builtin_amdgcn_exp2f(z), -1.4426950408889634f, -1.4426950408889634f));
      }
      g[t] = v2u{pk(o[0], o[1]), pk(o[2], o[3])};
    }
    const int gb = WS_G + (stile * 56 + (cn >> 1) * 7) * 1024 + lane * 16;
#pragma unroll
    for (int kk = 0; kk < 3; ++kk)
      __builtin_amdgcn_raw_buffer_store_b128(v4u{g[2 * kk].x, g[2 * kk].y, g[2 * kk + 1].x, g[2 * kk + 1].y}, rs, gb + (3 * (cn & 1) + kk) * 1024, 0, SC1);
    __builtin_amdgcn_raw_buffer_store_b64(g[6], rs, gb + 6 * 1024 + (cn & 1) * 8, 0, SC1);
    FENCE();
    SB0();
    STAMP(13);
    DRAIN();
    if (arrive(CT + C_CE + HALF, 3, lane) && lane == 0) __hip_atomic_store((GAS unsigned*)(ws + WS_FC + HALF * 8192 + pm * 512 + cn * 32), tag, RLX_AGENT);
    STAMP(14);
  }
  {
    if (HALF == 0 && w == 0) {
      delay(DCE);
      polln<16>(rs, WS_FC + (rt >> 1) * 512, tag, lane);
      if (lane == 0) __hip_atomic_store(CT + R_CE, 1u, RLX_WG);
    } else {
      lds_wait(CT + R_CE, 1u);
    }
    STAMP(15);
    const int row = tid >> 4, cq = tid & 15;
    LAS char* pw = L + w * 8192 + l15 * 256;
    LAS const char* pr = L + row * 256 + ((cq ^ (row & 7)) * 16);
    auto gload = [&](v4u (&gf)[2][7], v2u& hres, int hf) {
#pragma unroll
      for (int sg = 0; sg < 2; ++sg)
#pragma unroll
        for (int kk = 0; kk < 7; ++kk)
          gf[sg][kk] = __builtin_amdgcn_raw_buffer_load_b128(rs, WS_G + ((64 * hf + 2 * rt + sg) * 56 + w * 7) * 1024 + lane * 16, kk * 1024, SC1);
#pragma unroll
      for (int i = 0; i < 2; ++i)
        hres[i] = __builtin_amdgcn_raw_buffer_load_b32(rs, WS_HP + (64 * hf + 2 * rt + (row >> 4)) * 16384 + hslot(ct * 32 + cq * 2 + i) * 64 + (row & 15) * 4, 0, SC1);
      SB0();
    };
    auto gemm = [&](v4u (&gf)[2][7], int hf) {
#pragma unroll
      for (int sg = 0; sg < 2; ++sg) {
        f4 acc[4];
#pragma unroll
        for (int kk = 0; kk < 7; ++kk)
#pragma unroll
          for (int t = 0; t < 4; ++t) acc[t] = mfma(w2f[kk * 4 + t], gf[sg][kk], kk ? acc[t] : z4);
        if (hf == 0 && sg == 0) lds_wait(CT + C_W1, 8u);
#pragma unroll
        for (int e = 0; e < 4; ++e)
          *(LAS f4*)(pw + hf * 65536 + sg * 4096 + (((4 * lq + e) ^ (l15 & 7)) * 16)) = f4{acc[0][e], acc[1][e], acc[2][e], acc[3][e]};
      }
    };
    auto reduce = [&](v2u hres, int hf) {
      f4 s[8];
#pragma unroll
      for (int ww = 0; ww < 8; ++ww) s[ww] = *(LAS const f4*)(pr + hf * 65536 + ww * 8192);
      const f4 r = ((s[0] + s[1]) + (s[2] + s[3])) + ((s[4] + s[5]) + (s[6] + s[7]));
      v2u o;
      o.x = pk(lo_f(hres.x) + rbf(r[0]), hi_f(hres.x) + rbf(r[1]));
      o.y = pk(lo_f(hres.y) + rbf(r[2]), hi_f(hres.y) + rbf(r[3]));
      *(v2u*)(gout + (size_t)(hf * 1024 + rt * 32 + row) * 1024 + ct * 128 + cq * 8) = o;
    };
    v4u gf[2][7]; v2u h0, h1;
    gload(gf, h0, 0);
    gemm(gf, 0);
    STAMP(16);
    if (HALF == 0 && w == 0) polln<16>(rs, WS_FC + 8192 + (rt >> 1) * 512, tag, lane);
    BARRIER();
    STAMP(17);
    gload(gf, h1, 1);
    reduce(h0, 0);
    SB0();
    gemm(gf, 1);
    STAMP(18);
    BARRIER();
    reduce(h1, 1);
  }
  STAMP(19);
}

__global__ void __launch_bounds__(512, 2)
fused_kernel(const unsigned* __restrict__ gk, const unsigned* __restrict__ gv, const unsigned* __restrict__ gq,
             const unsigned* __restrict__ gsk, char* ws, const unsigned* __restrict__ gfil,
             const char* __restrict__ gw1, const char* __restrict__ gw2, const unsigned* __restrict__ gD, char* __restrict__ gout) {
  extern __shared__ __attribute__((aligned(16))) char smem_[];
  LAS char* L = (LAS char*)smem_;
  LAS unsigned* CT = (LAS unsigned*)(L + L_CT);
  const int b = __builtin_amdgcn_workgroup_id_x(), tid = __builtin_amdgcn_workitem_id_x(), lane = tid & 63;
  const int w = __builtin_amdgcn_readfirstlane(tid >> 6);
  const int xg = b & 7, idx = b >> 3;
  const __amdgpu_buffer_rsrc_t rs = __builtin_amdgcn_make_buffer_rsrc(ws, 0, WS_END, 0x00020000);
  STAMP(0);
  const int pair = xg * 32 + idx;
  const int cn = 8 * (xg & 1) + (idx & 7);
  const int rb = xg * 4 + (idx >> 3), cg = idx & 7;
  const int l15 = lane & 15, lq = lane >> 4;
  const int rot1 = ROT1 ? 4 * (idx >> 3) : 0;
  auto aload = [&](v4u (&A)[8][2]) {
    LAS const char* abase = L + L_HK + ((w >> 2) * 4 + ((l15 + 1) & 3)) * HKS + 2 * (((l15 + 1) & ~3) + 8 * lq + 64 * (w & 3));
#pragma unroll
    for (int m = 0; m < 8; ++m)
#pragma unroll
      for (int sp = 0; sp < 2; ++sp) {
        const v2u a0 = *(LAS const v2u*)(abase + 512 * m + 64 * sp), a1 = *(LAS const v2u*)(abase + 512 * m + 64 * sp + 8);
        A[m][sp] = v4u{a0.x, a0.y, a1.x, a1.y};
      }
  };
  auto epoch = [&]() { return *(const __attribute__((address_space(4))) unsigned*)(size_t)(ws + WS_EPOCH); };
  auto pin = [&](unsigned x) { asm volatile("" : "+s"(x)); return x; };
  auto qsload = [&](v4u& qv, v4u& st) {
    lds_wait(CT + R_A2, 1u);
    qv = __builtin_amdgcn_raw_buffer_load_b128(rs, WS_QT + pair * 8192 + tid * 16, 0, SC1);
    st = __builtin_amdgcn_raw_buffer_load_b128(rs, WS_STATS + tid * 16, 0, SC1);
  };
  const __amdgpu_buffer_rsrc_t rq = __builtin_amdgcn_make_buffer_rsrc((void*)gq, 0, 2 << 20, 0x00020000);
  unsigned so[7];
  if (w < 4) {
    const __amdgpu_buffer_rsrc_t rk = __builtin_amdgcn_make_buffer_rsrc((void*)gk, 0, 2 << 20, 0x00020000);
    const __amdgpu_buffer_rsrc_t rv = __builtin_amdgcn_make_buffer_rsrc((void*)gv, 0, 2 << 20, 0x00020000);
    const int Rw = rb * 64 + (w >> 1) * 32;
    const int pc = cg * 32 + (w & 1) * 16 + l15;
    const unsigned vo = (Rw + lq * 8) * 1024 + pc * 4;
    unsigned kd[11], vd[8], qd[8];
#pragma unroll
    for (int i = 0; i < 3; ++i) kd[i] = __builtin_amdgcn_raw_buffer_load_b32(rk, vo - 3072 + i * 1024, 0, 0);
#pragma unroll
    for (int i = 0; i < 8; ++i) kd[3 + i] = __builtin_amdgcn_raw_buffer_load_b32(rk, vo + (i & 3) * 1024, (i >> 2) * 4096, 0);
#pragma unroll
    for (int i = 0; i < 8; ++i) vd[i] = __builtin_amdgcn_raw_buffer_load_b32(rv, vo + (i & 3) * 1024, (i >> 2) * 4096, 0);
    const v4u skd = *(const v4u*)(gsk + pc * 4);
#pragma unroll
    for (int i = 0; i < 8; ++i) qd[i] = __builtin_amdgcn_raw_buffer_load_b32(rq, vo + (i & 3) * 1024, (i >> 2) * 4096, 0);
    STAMP(1);
    BARRIER();
    const unsigned dd = gD[pair];
    const unsigned ep = epoch();
    const float a0 = lo_f(skd.x), a1 = hi_f(skd.x), a2 = lo_f(skd.y), a3 = hi_f(skd.y);
    const float b0 = lo_f(skd.z), b1 = hi_f(skd.z), b2 = lo_f(skd.w), b3 = hi_f(skd.w);
    float x0[8], x1[8];
#pragma unroll
    for (int i = 0; i < 8; ++i) {
      float s0 = lo_f(kd[i]) * a3; s0 = __builtin_fmaf(lo_f(kd[i + 1]), a2, s0); s0 = __builtin_fmaf(lo_f(kd[i + 2]), a1, s0); s0 = __builtin_fmaf(lo_f(kd[i + 3]), a0, s0);
      float s1 = hi_f(kd[i]) * b3; s1 = __builtin_fmaf(hi_f(kd[i + 1]), b2, s1); s1 = __builtin_fmaf(hi_f(kd[i + 2]), b1, s1); s1 = __builtin_fmaf(hi_f(kd[i + 3]), b0, s1);
      x0[i] = s0 * lo_f(vd[i]); x1[i] = s1 * hi_f(vd[i]);
    }
    LAS char* tb = L + L_XS + w * 2560;
    const v4u c0 = {pk(x0[7], x0[6]), pk(x0[5], x0[4]), pk(x0[3], x0[2]), pk(x0[1], x0[0])};
    const v4u c1 = {pk(x1[7], x1[6]), pk(x1[5], x1[4]), pk(x1[3], x1[2]), pk(x1[1], x1[0])};
    *(LAS v4u*)(tb + (2 * l15) * 80 + (3 - lq) * 16) = c0;
    *(LAS v4u*)(tb + (2 * l15 + 1) * 80 + (3 - lq) * 16) = c1;
    FENCE();
    const int ch0 = cg * 64 + (w & 1) * 32;
    {
      const int ch = lane >> 2, cx = lane & 3;
      const v4u o0 = *(LAS const v4u*)(tb + ch * 80 + cx * 16);
      const v4u o1 = *(LAS const v4u*)(tb + (ch + 16) * 80 + cx * 16);
      FENCE();
      __builtin_amdgcn_raw_buffer_store_b128(o0, rs, WS_XT + (ch0 + ch) * 4096 + (2016 - Rw) * 2 + cx * 16, 0, SC1);
      __builtin_amdgcn_raw_buffer_store_b128(o1, rs, WS_XT + (ch0 + ch + 16) * 4096 + (2016 - Rw) * 2 + cx * 16, 0, SC1);
      FENCE();
      STAMP(2);
    }
    *(LAS v4u*)(tb + l15 * 144 + lq * 32) = v4u{qd[0], qd[1], qd[2], qd[3]};
    *(LAS v4u*)(tb + l15 * 144 + lq * 32 + 16) = v4u{qd[4], qd[5], qd[6], qd[7]};
    FENCE();
#pragma unroll
    for (int n = 0; n < 2; ++n) {
      const int pp = n * 8 + (lane >> 3), cx = lane & 7;
      const v4u o = *(LAS const v4u*)(tb + pp * 144 + cx * 16);
      __builtin_amdgcn_raw_buffer_store_b128(o, rs, WS_QT + (ch0 / 2 + pp) * 8192 + Rw * 4 + cx * 16, 0, SC1);
    }
    FENCE();
    (void)arrive(CT + C_XS, 0, lane);
    const unsigned tag = pin(ep + 1u);
    VMCNT(2);
    STAMP(3);
    if (arrive(CT + C_A1, 3, lane) && lane < 2) __hip_atomic_store((GAS unsigned*)(ws + WS_FA1 + lane * 8192 + cg * 1024 + rb * 32), tag, RLX_AGENT);
    DRAIN();
    if (arrive(CT + C_A2, 5, lane)) {
      if (lane < 8) __hip_atomic_store((GAS unsigned*)(ws + WS_FA2 + lane * 4096 + b * 16), tag, RLX_AGENT);
      if (lane == 0) __hip_atomic_store(CT + F_A, 1u, RLX_WG);
    }
    asm volatile("" :: "s"(dd), "s"(tag));
    STAMP(4);
    v4u A[8][2], qv, st;
    lds_wait(CT + C_HK, 2u);
    aload(A);
    if (w < 2) {
      delay(DA1);
      polln<32>(rs, WS_FA1 + w * 8192 + xg * 1024, tag, lane);
      if (lane == 0) __hip_atomic_store(CT + R_A1, 1u, RLX_WG);
      STAMP(5);
      v4u xa[4];
#pragma unroll
      for (int n = 0; n < 4; ++n) xa[n] = __builtin_amdgcn_raw_buffer_load_b128(rs, WS_XT + (2 * pair + w) * 4096 + n * 1024 + lane * 16, 0, SC1);
      lds_wait(CT + C_XS, 4u);
#pragma unroll
      for (int n = 0; n < 4; ++n) *(LAS v4u*)(L + L_XS + w * XSS + n * 1024 + lane * 16) = xa[n];
    } else if (w == 2) {
      lds_wait(CT + R_A1, 1u);
      poll256(rs, WS_FA2 + xg * 4096, tag, lane);
      if (lane == 0) __hip_atomic_store(CT + R_A2, 1u, RLX_WG);
      STAMP(5);
    } else {
      lds_wait(CT + C_XS, 4u);
      if (lane < 60) *(LAS v4u*)(L + L_XS + (lane >= 30 ? XSS - 480 : 0) + 4096 + lane * 16) = v4u{0u, 0u, 0u, 0u};
    }
    qsload(qv, st);
    BARRIER();
    STAMP(6);
    rest<0>(L, rs, ws, gout, b, tid, w, tag, dd, rot1, A, qv, st, nullptr, so STAMP_PASS);
  } else {
    unsigned dd, tag;
#pragma unroll
    for (int p = 0; p < 7; ++p) { const int u = p * 64 + lane, row = (u * 4682) >> 16, cc = u - row * 14; so[p] = row * 3584 + cc * 16; }
    if (w >= 6) {
      if (lane < C_N) CT[lane] = 0u;
      BARRIER();
      STAMP(1);
      dd = gD[pair];
      tag = epoch() + 1u;
      __builtin_amdgcn_s_sleep(2);
#pragma unroll 1
      for (int g = w & 1; g < 12; g += 2) {
        if (g >= W1E) { if ((g >> 1) == (W1E >> 1)) { lds_wait(CT + F_A, 1u); STAMP(3); } else VMCNT(7); }
        const char* src = gw1 + cn * 224 + ((g + rot1) & 15) * (32 * 3584);
        const unsigned dst = (unsigned)(size_t)(L + L_W1) + g * 7168;
#pragma unroll
        for (int p = 0; p < 7; ++p) glds16(src, so[p], dst + p * 1024);
      }
      tag = pin(tag);
      STAMP(2);
    } else {
      const int wb = w - 4;
      const int unit = b * 2 + wb;
      const bool do_w2 = b < 224;
      const int uc = unit / 56, ur = unit - uc * 56, ukr = ur / 7, kk = ur - ukr * 7;
      const int f0 = kk < 3 ? 32 * kk : kk < 6 ? 112 + 32 * (kk - 3) : 96, fs = kk < 6 ? 16 : 112;
      const int base = (224 * ukr + f0 + 4 * lq) * 1024 + uc * 128 + l15 * 8;
      const int srow = xg * 256 + idx * 8 + 4 * wb + lq;
      const __amdgpu_buffer_rsrc_t rw2 = __builtin_amdgcn_make_buffer_rsrc((void*)gw2, 0, do_w2 ? 1792 * 1024 : 0, 0x00020000);
      const __amdgpu_buffer_rsrc_t rw2t = __builtin_amdgcn_make_buffer_rsrc(ws, 0, do_w2 ? WS_END : 0, 0x00020000);
      const __amdgpu_buffer_rsrc_t rf = __builtin_amdgcn_make_buffer_rsrc((void*)(gfil + (2 * pair + wb) * 1024), 0, 4096, 0x00020000);
      dd = gD[pair];
      tag = epoch() + 1u;
      BARRIER();
      STAMP(1);
      v4u fd[4]; v2u fn[4];
#pragma unroll
      for (int n = 0; n < 4; ++n) {
        fd[n] = __builtin_amdgcn_raw_buffer_load_b128(rf, lane * 16 + n * 1024, 0, 0);
        fn[n] = __builtin_amdgcn_raw_buffer_load_b64(rf, lane * 16 + n * 1024 + 16, 0, 0);
      }
      v4u r[4];
#pragma unroll
      for (int i = 0; i < 4; ++i) r[i] = __builtin_amdgcn_raw_buffer_load_b128(rq, srow * 1024 + l15 * 16 + i * 256, 0, 0);
      v2u w2r[8];
#pragma unroll
      for (int j = 0; j < 8; ++j) w2r[j] = __builtin_amdgcn_raw_buffer_load_b64(rw2, base + (j & 3) * 1024, (j >> 2) * fs * 1024, 0);
      SB0();
      LAS char* hk = L + L_HK + wb * 4 * HKS;
#pragma unroll
      for (int n = 0; n < 4; ++n) {
        LAS char* p = hk + 32 + (n * 64 + lane) * 16;
        const unsigned s1 = ab(fd[n].y, fd[n].x), s2 = ab(fd[n].z, fd[n].y), s3 = ab(fd[n].w, fd[n].z), s4_ = ab(fn[n].x, fd[n].w), s5 = ab(fn[n].y, fn[n].x);
        *(LAS v4u*)(p) = fd[n];
        *(LAS v4u*)(p + HKS) = v4u{s1, s2, s3, s4_};
        *(LAS v4u*)(p + 2 * HKS) = v4u{fd[n].y, fd[n].z, fd[n].w, fn[n].x};
        *(LAS v4u*)(p + 3 * HKS) = v4u{s2, s3, s4_, s5};
      }
      {
        const unsigned d0 = __builtin_amdgcn_readfirstlane(fd[0].x), d1 = __builtin_amdgcn_readfirstlane(fd[0].y);
        const int c = lane >> 1;
        const unsigned z = lane == 7 ? d0 << 16 : 0u;
        const unsigned ww = lane == 3 ? d0 << 16 : lane == 5 ? d0 : lane == 7 ? ab(d1, d0) : 0u;
        if (lane < 8) *(LAS v4u*)(hk + c * HKS + (lane & 1) * 16) = v4u{0u, 0u, z, ww};
      }
      LGKM0();
      tag = pin(tag);
      (void)arrive(CT + C_HK, 0, lane);
      STAMP(2);
      SB0();
      {
        const unsigned one = 0x3f803f80u;
        float s0 = 0.f, q0 = 0.f, s1 = 0.f, q1 = 0.f;
#pragma unroll
        for (int i = 0; i < 4; ++i) {
          s0 = dot2(r[i].x, one, s0); q0 = dot2(r[i].x, r[i].x, q0); s1 = dot2(r[i].y, one, s1); q1 = dot2(r[i].y, r[i].y, q1);
          s0 = dot2(r[i].z, one, s0); q0 = dot2(r[i].z, r[i].z, q0); s1 = dot2(r[i].w, one, s1); q1 = dot2(r[i].w, r[i].w, q1);
        }
        s0 += s1; q0 += q1;
        s0 = dpp_add<0xB1>(s0); q0 = dpp_add<0xB1>(q0); s0 = dpp_add<0x4E>(s0); q0 = dpp_add<0x4E>(q0);
        s0 = dpp_add<0x141>(s0); q0 = dpp_add<0x141>(q0); s0 = dpp_add<0x140>(s0); q0 = dpp_add<0x140>(q0);
        float m, d;
        ln_fin(s0, q0, m, d);
        const unsigned sd = pk(m, d);
        v4u o[4];
#pragma unroll
        for (int t = 0; t < 4; ++t)
#pragma unroll
          for (int i = 0; i < 4; ++i) o[t][i] = __builtin_amdgcn_perm(w2r[2 * i + 1][t >> 1], w2r[2 * i][t >> 1], (t & 1) ? 0x07060302u : 0x05040100u);
        SB0();
        STAMP(5);
        const v4u s4v = {(unsigned)__builtin_amdgcn_readlane(sd, 0), (unsigned)__builtin_amdgcn_readlane(sd, 16), (unsigned)__builtin_amdgcn_readlane(sd, 32), (unsigned)__builtin_amdgcn_readlane(sd, 48)};
        if (lane == 0) __builtin_amdgcn_raw_buffer_store_b128(s4v, rs, WS_STATS + srow * 4, 0, SC1);
#pragma unroll
        for (int t = 0; t < 4; ++t) __builtin_amdgcn_raw_buffer_store_b128(o[t], rw2t, WS_W2 + (unit * 4 + t) * 1024 + lane * 16, 0, SC1);
      }
      DRAIN();
      STAMP(3);
      if (arrive(CT + C_A2, 5, lane)) {
        if (lane < 8) __hip_atomic_store((GAS unsigned*)(ws + WS_FA2 + lane * 4096 + b * 16), tag, RLX_AGENT);
        if (lane == 0) __hip_atomic_store(CT + F_A, 1u, RLX_WG);
      }
    }
    asm volatile("" :: "s"(dd), "s"(tag));
    STAMP(4);
    lds_wait(CT + C_HK, 2u);
    v4u A[8][2], qv, st;
    aload(A);
    qsload(qv, st);
    BARRIER();
    STAMP(6);
    rest<1>(L, rs, ws, gout, b, tid, w, tag, dd, rot1, A, qv, st, gw1 + cn * 224 + ((8 + w + rot1) & 15) * (32 * 3584), so STAMP_PASS);
  }
}

#ifdef __HIP_DEVICE_COMPILE__
#pragma clang attribute pop
#endif
extern "C" void kernel_launch(void* const* d_in, const int* in_sizes, int n_in, void* d_out, int out_size, void* d_ws, size_t ws_size, hipStream_t stream) {
  (void)hipFuncSetAttribute((const void*)fused_kernel, hipFuncAttributeMaxDynamicSharedMemorySize, 163840);
  fused_kernel<<<256, 512, 163840, stream>>>((const unsigned*)d_in[1], (const unsigned*)d_in[2], (const unsigned*)d_in[0],
      (const unsigned*)d_in[3], (char*)d_ws, (const unsigned*)d_in[4], (const char*)d_in[7], (const char*)d_in[8],
      (const unsigned*)d_in[5], (char*)d_out);
}
```

```cpp
#include <hip/hip_runtime.h>
#include <stdint.h>
constexpr int WS_STAMP = 10485760;

__global__ void __launch_bounds__(512, 2)
fused_kernel(const char* gk, const char* gv, const char* gq, const char* gsk, char* ws, const char* gfil,
             const char* gw1, const char* gw2, const char* gD, char* gout) {
#ifdef __HIP_DEVICE_COMPILE__
  asm volatile(
    "s_load_dwordx16 s[8:23], %0, 0x0\n"
    "s_load_dwordx4 s[24:27], %0, 0x40\n"
    "s_getpc_b64 s[48:49]\n"
    ".Lcode_a:\n"
    "s_and_b32 s50, %1, 0x18\n"
    "s_lshl_b32 s50, s50, 10\n"
    "v_lshlrev_b32 v3, 4, v0\n"
    "v_and_b32 v3, 0x1f80, v3\n"
    "v_add_u32 v3, s50, v3\n"
    "v_min_u32 v3, .Lcode_b-.Lcode_a-4, v3\n"
    "global_load_dword v4, v3, s[48:49]\n"
    "v_lshrrev_b32 v16, 6, v0\n"
    "s_and_b32 s31, %1, 7\n"
    "s_lshr_b32 s30, %1, 3\n"
    "v_readfirstlane_b32 s28, v16\n"
    "v_and_b32 v16, 6, v16\n"
    "v_bfe_u32 v17, v0, 4, 1\n"
    "v_xor_b32 v16, v16, v0\n"
    "v_xor_b32 v16, v16, v17\n"
    "v_and_b32 v16, 7, v16\n"
    "v_lshlrev_b32 v16, 4, v16\n"
    "v_lshrrev_b32 v17, 3, v0\n"
    "v_lshl_add_u32 v20, v17, 10, v16\n"
    "s_add_u32 s45, s31, 2\n"
    "s_and_b32 s45, s45, 7\n"
    "s_lshl_b32 s50, s30, 16\n"
    "s_lshl_b32 s51, s45, 7\n"
    "s_add_u32 s50, s50, s51\n"
    "v_add_u32 v20, s50, v20\n"
    "s_lshl_b32 s58, s28, 10\n"
    "s_add_u32 m0, s58, 1024\n"
    "s_lshl_b32 s29, s31, 5\n"
    "s_add_u32 s29, s29, s30\n"
    "v_and_b32 v1, 63, v0\n"
    "v_lshlrev_b32 v1, 4, v1\n"
    "v_subrev_u32 v18, 0x10000, v20\n"
    "v_max_i32 v18, 0, v18\n"
    "s_lshl_b32 s51, s45, 3\n"
    "s_add_u32 s51, s51, s28\n"
    "s_lshl_b32 s52, s51, 6\n"
    "s_cmp_eq_u32 s28, 7\n"
    "s_cselect_b32 s54, 0xffffff00, 0\n"
    "s_cmp_eq_u32 s30, 0\n"
    "s_cselect_b32 s55, s54, 0\n"
    "s_cselect_b32 s54, 0, s54\n"
    "s_lshr_b32 s53, s29, 1\n"
    "s_xor_b32 s53, s53, s29\n"
    "s_and_b32 s53, s53, 16\n"
    "s_mul_i32 s53, s53, 3\n"
    "s_xor_b32 s39, s29, s53\n"
    "s_lshr_b32 s36, s28, 2\n"
    "s_lshl_b32 s53, s39, 1\n"
    "s_add_u32 s53, s53, s36\n"
    "s_lshl_b32 s53, s53, 12\n"
    "s_waitcnt lgkmcnt(0)\n"
    "global_load_lds_dwordx4 v20, s[8:9]\n"
    "s_add_u32 m0, s58, 9216\n"
    "s_load_dwordx16 s[84:99], s[14:15], s52\n"
    "global_load_lds_dwordx4 v20, s[10:11]\n"
    "s_load_dword s37, s[16:17], 0x0\n"
    "s_lshl_b32 s52, s39, 2\n"
    "s_load_dword s38, s[24:25], s52\n"
    "s_mov_b32 m0, 0\n"
    "s_mov_b32 exec_lo, 0\n"
    "s_mov_b32 exec_hi, s54\n"
    "global_load_lds_dwordx4 v18, s[8:9]\n"
    "s_mov_b64 exec, -1\n"
    "s_cmp_lg_u32 s28, 0\n"
    "s_cbranch_scc1 .Lot0\n"
    "s_lshr_b32 s48, s31, 1\n"
    "s_mul_i32 s48, s48, 896\n"
    "s_and_b32 s49, s31, 1\n"
    "s_mul_i32 s49, s49, 416\n"
    "s_add_u32 s48, s48, s49\n"
    "s_mul_i32 s49, s30, 57344\n"
    "s_add_u32 s48, s48, s49\n"
    "v_lshrrev_b32 v232, 1, v0\n"
    "v_mul_u32_u24 v232, 3552, v232\n"
    "v_add3_u32 v232, v232, v1, s48\n"
    "s_mov_b32 exec_hi, 0\n"
    "global_load_dwordx4 v[232:235], v232, s[20:21] offset:224\n"
    ".Lot0:\n"
    "s_mov_b64 exec, -1\n"
    "s_mov_b32 s4, s16\n"
    "s_mov_b32 s5, s17\n"
    "s_mov_b32 s6, 10485760\n"
    "s_mov_b32 s7, 0x00020000\n"
    "s_add_u32 s16, s18, s53\n"
    "s_addc_u32 s17, s19, 0\n"
    "s_mov_b32 s18, 4096\n"
    "s_mov_b32 s19, 0x00020000\n"
    "v_and_b32 v15, 0xff, v0\n"
    "v_lshlrev_b32 v15, 4, v15\n"
    "v_subrev_u32 v18, 8, v15\n"
    "v_and_b32 v18, 0x7fffffff, v18\n"
    "s_and_b32 s52, s28, 3\n"
    "s_barrier\n"
    "s_lshr_b32 s53, s29, 1\n"
    "s_lshl_b32 s53, s53, 2\n"
    "s_add_u32 s53, s53, s52\n"
    "s_lshl_b32 s53, s53, 1\n"
    "s_and_b32 s54, s29, 1\n"
    "s_add_u32 s53, s53, s54\n"
    "s_lshl_b32 s53, s53, 1\n"
    "s_add_u32 s53, s53, s36\n"
    "s_lshl_b32 s53, s53, 10\n"
    "v_add_u32 v21, s53, v1\n"
    "global_load_dwordx4 v[92:95], v21, s[12:13]\n"
    "buffer_load_dwordx4 v[82:85], v15, s[16:19], 0 offen\n"
    "buffer_load_dwordx2 v[80:81], v18, s[16:19], 0 offen\n"
    "v_mov_b32 v24, 0\n"
    "v_mov_b32 v25, 0\n"
    "v_mov_b32 v26, 0\n"
    "v_mov_b32 v27, 0\n"
    "s_mov_b32 exec_lo, 0\n"
    "s_mov_b32 exec_hi, s55\n"
    "ds_write_b128 v1, v[24:27] offset:0\n"
    "s_mov_b64 exec, -1\n"
    "v_and_b32 v22, 15, v0\n"
    "v_bfe_u32 v23, v0, 4, 2\n"
    "v_and_b32 v17, 3, v0\n"
    "v_bfe_u32 v18, v0, 2, 2\n"
    "v_lshl_add_u32 v18, v18, 2, v23\n"
    "v_lshl_add_u32 v18, v18, 2, v17\n"
    "s_lshr_b32 s44, s51, 1\n"
    "s_xor_b32 s44, s44, s51\n"
    "s_and_b32 s44, s44, 4\n"
    "s_mul_i32 s44, s44, 3\n"
    "s_xor_b32 s44, s44, s51\n"
    "s_mul_i32 s44, s44, 114688\n"
    "s_lshr_b32 s49, s30, 2\n"
    "s_mul_i32 s49, s49, 3584\n"
    "s_add_u32 s49, s49, s44\n"
    "s_add_u32 s49, s49, 3145728\n"
    "s_and_b32 s50, s30, 3\n"
    "s_lshl_b32 s50, s50, 7\n"
    "s_add_u32 s9, s49, s50\n"
    "s_add_u32 s8, s9, s50\n"
    "s_mov_b32 s40, 0\n"
    "s_mov_b32 s41, 0x05040100\n"
    "s_mov_b32 s42, 0x07060302\n"
    "s_mov_b32 s60, 0xffff0000\n"
    "v_lshrrev_b32 v104, 3, v18\n"
    "v_bfe_u32 v19, v18, 1, 1\n"
    "v_and_or_b32 v104, v104, 6, v19\n"
    "v_xor_b32 v104, s28, v104\n"
    "v_lshlrev_b32 v104, 4, v104\n"
    "v_lshl_add_u32 v104, v18, 7, v104\n"
    "v_subrev_u32 v3, 1, v18\n"
    "v_lshrrev_b32 v105, 3, v3\n"
    "v_bfe_u32 v19, v3, 1, 1\n"
    "v_and_or_b32 v105, v105, 6, v19\n"
    "v_xor_b32 v105, s28, v105\n"
    "v_lshlrev_b32 v105, 4, v105\n"
    "v_lshl_add_u32 v105, v3, 7, v105\n"
    "v_add_u32 v105, 1024, v105\n"
    "v_subrev_u32 v3, 2, v18\n"
    "v_lshrrev_b32 v106, 3, v3\n"
    "v_bfe_u32 v19, v3, 1, 1\n"
    "v_and_or_b32 v106, v106, 6, v19\n"
    "v_xor_b32 v106, s28, v106\n"
    "v_lshlrev_b32 v106, 4, v106\n"
    "v_lshl_add_u32 v106, v3, 7, v106\n"
    "v_add_u32 v106, 1024, v106\n"
    "v_subrev_u32 v3, 3, v18\n"
    "v_lshrrev_b32 v107, 3, v3\n"
    "v_bfe_u32 v19, v3, 1, 1\n"
    "v_and_or_b32 v107, v107, 6, v19\n"
    "v_xor_b32 v107, s28, v107\n"
    "v_lshlrev_b32 v107, 4, v107\n"
    "v_lshl_add_u32 v107, v3, 7, v107\n"
    "v_add_u32 v107, 1024, v107\n"
    "v_mul_u32_u24 v109, 28672, v23\n"
    "v_lshl_add_u32 v108, v22, 4, v109\n"
    "v_lshl_add_u32 v109, v22, 3, v109\n"
    "s_lshr_b32 s48, s30, 3\n"
    "s_and_b32 s35, s30, 7\n"
    "s_and_b32 s32, s31, 1\n"
    "s_lshl_b32 s32, s32, 3\n"
    "s_add_u32 s32, s32, s35\n"
    "s_add_u32 s35, s35, 0\n"
    "s_and_b32 s35, s35, 7\n"
    "v_mov_b32 v100, -1\n"
    "v_mov_b32 v101, -1\n"
    "v_mov_b32 v102, -1\n"
    "v_mov_b32 v103, -1\n"
    "s_lshr_b32 s33, s31, 1\n"
    "s_lshl_b32 s33, s33, 2\n"
    "s_add_u32 s33, s33, s48\n"
    "s_lshl_b32 s33, s33, 2\n"
    "s_lshl_b32 s49, s36, 6\n"
    "s_add_u32 s33, s33, s49\n"
    "s_lshl_b32 s49, s33, 2\n"
    "s_add_u32 s49, s49, s32\n"
    "s_lshl_b32 s49, s49, 2\n"
    "s_add_u32 s49, s49, s52\n"
    "s_lshl_b32 s49, s49, 10\n"
    "s_add_u32 s10, s49, 1048576\n"
    "buffer_store_dwordx4 v[100:103], v1, s[4:7], s10 offen sc1\n"
    "s_add_u32 s33, s33, s52\n"
    "s_lshl_b32 s46, s33, 11\n"
    "s_add_u32 s46, s46, 65536\n"
    "s_lshl_b32 s49, s32, 7\n"
    "s_add_u32 s11, s49, s46\n"
    "s_mov_b64 exec, 0xff\n"
    "buffer_store_dwordx4 v[100:103], v1, s[4:7], s11 offen sc1\n"
    "s_mov_b64 exec, -1\n"
    "v_mov_b32 v2, 158208\n"
    "s_mul_i32 s53, s36, 4672\n"
    "s_add_u32 s50, s53, 147968\n"
    "v_add_u32 v3, s50, v1\n"
    "v_add_u32 v19, v2, v1\n"
    "s_mov_b32 exec_lo, 0x3fffffff\n"
    "s_mov_b32 exec_hi, 0\n"
    "ds_write_b128 v3, v[24:27] offset:4096\n"
    "s_mov_b64 exec, 0xff\n"
    "ds_write_b128 v19, v[24:27]\n"
    "s_mov_b64 exec, -1\n"
    "s_mul_i32 s53, s36, 16640\n"
    "s_add_u32 s53, s53, 114688\n"
    "v_add_u32 v112, s53, v15\n"
    "v_bfe_u32 v114, v0, 1, 3\n"
    "v_mul_u32_u24 v114, 4160, v114\n"
    "v_and_b32 v3, 1, v0\n"
    "v_lshl_add_u32 v114, v3, 4, v114\n"
    "v_add_u32 v114, 114688, v114\n"
    "s_lshl_b32 s51, s52, 7\n"
    "s_add_u32 s53, s53, s51\n"
    "s_add_u32 s53, s53, 12488\n"
    "v_lshrrev_b32 v15, 2, v22\n"
    "v_mul_u32_u24 v15, 4160, v15\n"
    "v_sub_u32 v15, s53, v15\n"
    "v_lshl_add_u32 v15, v17, 3, v15\n"
    "v_lshl_add_u32 v15, v23, 4, v15\n"
    "ds_write_b128 v114, v[24:27]\n"
    "s_add_u32 s53, s50, s51\n"
    "s_add_u32 s53, s53, 480\n"
    "v_lshlrev_b32 v6, 5, v22\n"
    "v_sub_u32 v6, s53, v6\n"
    "v_lshl_add_u32 v6, v23, 4, v6\n"
    "v_lshrrev_b32 v16, 2, v0\n"
    "v_lshlrev_b32 v12, 5, v16\n"
    "v_lshl_add_u32 v12, v17, 1, v12\n"
    "v_sub_u32 v12, 152038, v12\n"
    "s_lshl_b32 s44, s29, 3\n"
    "s_add_u32 s44, s44, s28\n"
    "s_mul_i32 s44, s44, 3584\n"
    "s_add_u32 s44, s44, 3145728\n"
    "v_lshrrev_b32 v230, 1, v1\n"
    "v_and_b32 v113, 0x1fc, v0\n"
    "v_lshl_add_u32 v113, v113, 22, v230\n"
    "v_and_b32 v3, 1, v0\n"
    "v_mul_u32_u24 v3, 1016, v3\n"
    "v_add_u32 v113, v113, v3\n"
    "s_lshl_b32 s14, s31, 5\n"
    "s_add_u32 s14, s14, s29\n"
    "s_lshl_b32 s14, s14, 5\n"
    "s_add_u32 s14, s14, 16384\n"
    "v_mov_b32 v86, 0x3f803f80\n"
    "v_lshl_add_u32 v87, s28, 2, v2\n"
    "v_add_u32 v110, v230, v2\n"
    "v_lshl_add_u32 v14, s36, 2, v2\n"
    "v_and_b32 v10, 63, v0\n"
    "v_mul_u32_u24 v10, 4682, v10\n"
    "v_lshrrev_b32 v10, 16, v10\n"
    "v_mul_u32_u24 v10, 3360, v10\n"
    "v_add_u32 v10, v10, v1\n"
    "s_lshl_b32 s15, s28, 1\n"
    "s_add_u32 s15, s15, s32\n"
    "s_lshl_b32 s15, s15, 1\n"
    "s_mul_i32 s54, s32, 224\n"
    "v_add_u32 v10, s54, v10\n"
    "s_waitcnt lgkmcnt(0)\n"
    "s_add_u32 s37, s37, 0x00010001\n"
    "s_or_b32 s37, s37, 0xc0000000\n"
    "v_mov_b32 v123, s37\n"
    "v_mov_b32 v125, s37\n"
    "v_mov_b32 v131, s37\n"
    "v_mov_b32 v135, s37\n"
    "v_mov_b32 v139, s37\n"
    "s_lshl_b32 s53, s38, 16\n"
    "s_and_b32 s38, s38, s60\n"
    "s_cmp_eq_u32 s36, 0\n"
    "s_cselect_b32 s38, s53, s38\n"
    "s_cmp_eq_u32 s52, 0\n"
    "s_cselect_b32 s38, s38, 0\n"
    "s_mov_b32 s16, s20\n"
    "s_mov_b32 s17, s21\n"
    "s_mov_b32 s18, 1835008\n"
    "s_mov_b32 s19, 0x00020000\n"
    "s_waitcnt vmcnt(5)\n"
    "s_barrier\n"
    "ds_read_b128 v[28:31], v104 offset:1024\n"
    "ds_read_b128 v[32:35], v105 offset:0\n"
    "ds_read_b128 v[36:39], v106 offset:0\n"
    "ds_read_b128 v[40:43], v107 offset:0\n"
    "ds_read_b128 v[44:47], v104 offset:9216\n"
    "s_mov_b32 s20, s22\n"
    "s_mov_b32 s21, s23\n"
    "s_mov_b32 s22, 1835008\n"
    "s_mov_b32 s23, 0x00020000\n"
    "s_waitcnt lgkmcnt(3)\n"
    "v_perm_b32 v60, v32, v28, s41\n"
    "v_perm_b32 v61, v32, v28, s42\n"
    "v_perm_b32 v62, v33, v29, s41\n"
    "v_perm_b32 v63, v33, v29, s42\n"
    "v_perm_b32 v64, v34, v30, s41\n"
    "v_perm_b32 v65, v34, v30, s42\n"
    "v_perm_b32 v66, v35, v31, s41\n"
    "v_perm_b32 v67, v35, v31, s42\n"
    "v_dot2_f32_bf16 v68, v60, s84, 0\n"
    "v_dot2_f32_bf16 v69, v61, s86, 0\n"
    "v_dot2_f32_bf16 v70, v62, s88, 0\n"
    "v_dot2_f32_bf16 v71, v63, s90, 0\n"
    "v_dot2_f32_bf16 v72, v64, s92, 0\n"
    "v_dot2_f32_bf16 v73, v65, s94, 0\n"
    "v_dot2_f32_bf16 v74, v66, s96, 0\n"
    "v_dot2_f32_bf16 v75, v67, s98, 0\n"
    "s_waitcnt lgkmcnt(1)\n"
    "v_perm_b32 v60, v40, v36, s41\n"
    "v_perm_b32 v61, v40, v36, s42\n"
    "v_perm_b32 v62, v41, v37, s41\n"
    "v_perm_b32 v63, v41, v37, s42\n"
    "v_perm_b32 v64, v42, v38, s41\n"
    "v_perm_b32 v65, v42, v38, s42\n"
    "v_perm_b32 v66, v43, v39, s41\n"
    "v_perm_b32 v67, v43, v39, s42\n"
    "v_dot2_f32_bf16 v68, v60, s85, v68\n"
    "v_dot2_f32_bf16 v69, v61, s87, v69\n"
    "v_dot2_f32_bf16 v70, v62, s89, v70\n"
    "v_dot2_f32_bf16 v71, v63, s91, v71\n"
    "v_dot2_f32_bf16 v72, v64, s93, v72\n"
    "v_dot2_f32_bf16 v73, v65, s95, v73\n"
    "v_dot2_f32_bf16 v74, v66, s97, v74\n"
    "v_dot2_f32_bf16 v75, v67, s99, v75\n"
    "s_waitcnt lgkmcnt(0)\n"
    "s_mov_b64 exec, 1\n"
    "ds_inc_u32 v2, v2 offset:0\n"
    "s_mov_b64 exec, -1\n"
    "v_lshlrev_b32 v52, 16, v44\n"
    "v_and_b32 v53, s60, v44\n"
    "v_lshlrev_b32 v54, 16, v45\n"
    "v_and_b32 v55, s60, v45\n"
    "v_lshlrev_b32 v56, 16, v46\n"
    "v_and_b32 v57, s60, v46\n"
    "v_lshlrev_b32 v58, 16, v47\n"
    "v_and_b32 v59, s60, v47\n"
    "v_pk_mul_f32 v[68:69], v[68:69], v[52:53]\n"
    "v_pk_mul_f32 v[70:71], v[70:71], v[54:55]\n"
    "v_pk_mul_f32 v[72:73], v[72:73], v[56:57]\n"
    "v_pk_mul_f32 v[74:75], v[74:75], v[58:59]\n"
    "v_cvt_pk_bf16_f32 v120, v68, v69\n"
    "v_cvt_pk_bf16_f32 v121, v70, v71\n"
    "v_cvt_pk_bf16_f32 v122, v72, v73\n"
    "v_cvt_pk_bf16_f32 v124, v74, v75\n"
    "s_nop 0\n"
    "v_permlane32_swap_b32 v120, v122\n"
    "v_permlane32_swap_b32 v121, v124\n"
    "s_nop 1\n"
    "v_permlane16_swap_b32 v120, v121\n"
    "v_permlane16_swap_b32 v122, v124\n"
    "buffer_store_dwordx4 v[120:123], v108, s[4:7], s8 offen sc1\n"
    "buffer_store_dwordx2 v[124:125], v109, s[4:7], s9 offen offset:3072 sc1\n"
    "s_cmp_lg_u32 s28, 0\n"
    "s_cbranch_scc1 .Lot1\n"
    "s_lshl_b32 s48, s29, 9\n"
    "s_add_u32 s48, s48, 524288\n"
    "s_mov_b32 exec_hi, 0\n"
    "buffer_store_dwordx4 v[232:235], v1, s[4:7], s48 offen sc1\n"
    "s_mov_b32 exec_hi, -1\n"
    ".Lot1:\n"
    "s_lshr_b32 s47, s30, 3\n"
    "s_mul_i32 s47, s47, 1\n"
    "s_add_u32 s59, s47, 3\n"
    "s_and_b32 s59, s59, 3\n"
    "s_mul_i32 s59, s59, 3584\n"
    "s_mul_i32 s61, s28, 14336\n"
    "s_or_b32 s53, s28, 2\n"
    "s_mul_i32 s53, s53, 14336\n"
    "s_add_u32 s53, s53, s59\n"
    "s_and_b32 s54, s28, 2\n"
    "s_lshl_b32 s54, s54, 9\n"
    "s_add_u32 m0, s53, s54\n"
    "s_nop 0\n"
    "global_load_lds_dwordx4 v20, s[12:13]\n"
    "v_lshrrev_b32 v16, 3, v18\n"
    "v_and_b32 v19, 2, v16\n"
    "v_or_b32 v16, 2, v16\n"
    "v_mul_u32_u24 v111, 14336, v16\n"
    "v_lshl_add_u32 v111, v19, 9, v111\n"
    "v_and_b32 v16, 0x3ff, v104\n"
    "v_add3_u32 v111, v111, v16, s59\n"
    "s_waitcnt vmcnt(5)\n"
    "s_mov_b64 exec, 1\n"
    "v_lshlrev_b32 v3, 16, v82\n"
    "v_add_f32 v3, s38, v3\n"
    "v_cvt_pk_bf16_f32 v3, v3, v3\n"
    "v_bfi_b32 v82, s60, v82, v3\n"
    "s_mov_b64 exec, -1\n"
    "ds_read_b32 v4, v2 offset:0\n"
    "v_dot2_f32_bf16 v88, v92, v86, 0\n"
    "v_dot2_f32_bf16 v89, v92, v92, 0\n"
    "v_dot2_f32_bf16 v90, v93, v86, 0\n"
    "v_dot2_f32_bf16 v91, v93, v93, 0\n"
    "v_dot2_f32_bf16 v88, v94, v86, v88\n"
    "v_dot2_f32_bf16 v89, v94, v94, v89\n"
    "v_dot2_f32_bf16 v90, v95, v86, v90\n"
    "v_dot2_f32_bf16 v91, v95, v95, v91\n"
    "ds_write_b128 v112, v[82:85] offset:32\n"
    "v_alignbit_b32 v28, v82, v81, 16\n"
    "v_alignbit_b32 v29, v83, v82, 16\n"
    "v_add_f32 v88, v88, v90\n"
    "v_add_f32 v89, v89, v91\n"
    "v_alignbit_b32 v30, v84, v83, 16\n"
    "v_alignbit_b32 v31, v85, v84, 16\n"
    "v_permlane32_swap_b32 v88, v89\n"
    "v_add_f32 v88, v88, v89\n"
    "ds_write_b128 v112, v[28:31] offset:4192\n"
    "v_mov_b32 v32, v81\n"
    "v_add_f32_dpp v88, v88, v88 row_ror:8 row_mask:0xf bank_mask:0xf\n"
    "v_mov_b32 v33, v82\n"
    "v_mov_b32 v34, v83\n"
    "v_add_f32_dpp v88, v88, v88 row_ror:4 row_mask:0xf bank_mask:0xf\n"
    "v_mov_b32 v35, v84\n"
    "ds_write_b128 v112, v[32:35] offset:8352\n"
    "v_add_f32_dpp v88, v88, v88 row_ror:2 row_mask:0xf bank_mask:0xf\n"
    "v_alignbit_b32 v36, v81, v80, 16\n"
    "v_alignbit_b32 v37, v82, v81, 16\n"
    "v_add_f32_dpp v88, v88, v88 row_ror:1 row_mask:0xf bank_mask:0xf\n"
    "v_mov_b32 v89, v88\n"
    "v_alignbit_b32 v38, v83, v82, 16\n"
    "v_alignbit_b32 v39, v84, v83, 16\n"
    "v_permlane16_swap_b32 v88, v89\n"
    "v_add_f32 v88, v88, v89\n"
    "v_mov_b32 v89, v88\n"
    "ds_write_b128 v112, v[36:39] offset:12512\n"
    "s_nop 0\n"
    "v_permlane32_swap_b32 v88, v89\n"
    "v_mul_f32 v88, 0x3b000000, v88\n"
    "v_mul_f32 v89, 0x3b000000, v89\n"
    "v_fma_f32 v89, -v88, v88, v89\n"
    "v_max_f32 v89, 0, v89\n"
    "v_cvt_pk_bf16_f32 v89, v89, v89\n"
    "v_lshlrev_b32 v89, 16, v89\n"
    "v_add_f32 v89, 0x3727c5ac, v89\n"
    "v_cvt_pk_bf16_f32 v89, v89, v89\n"
    "v_lshlrev_b32 v89, 16, v89\n"
    "v_sqrt_f32 v89, v89\n"
    "s_nop 0\n"
    "v_cvt_pk_bf16_f32 v136, v88, v89\n"
    "s_mov_b64 exec, 1\n"
    "ds_write_b32 v87, v136 offset:128\n"
    "s_mov_b64 exec, -1\n"
    "s_waitcnt vmcnt(0) lgkmcnt(0)\n"
    "s_barrier\n"
    "s_memrealtime s[52:53]\n"
    "ds_read_b64 v[128:129], v111\n"
    "ds_read_b64 v[132:133], v111 offset:8\n"
    "ds_read_b64 v[136:137], v110 offset:128\n"
    "ds_read_b64 v[16:17], v15 offset:0\n"
    "ds_read_b64 v[18:19], v15 offset:8\n"
    "ds_read_b64 v[20:21], v15 offset:64\n"
    "ds_read_b64 v[22:23], v15 offset:72\n"
    "s_waitcnt lgkmcnt(0)\n"
    "v_mov_b32 v138, s52\n"
    "buffer_store_dwordx4 v[136:139], v113, s[4:7], s14 offen sc1\n"
    "v_permlane32_swap_b32 v128, v132\n"
    "v_permlane32_swap_b32 v129, v133\n"
    "s_nop 1\n"
    "v_permlane16_swap_b32 v128, v129\n"
    "v_permlane16_swap_b32 v132, v133\n"
    "buffer_store_dwordx4 v[128:131], v108, s[4:7], s8 offen offset:1024 sc1\n"
    "buffer_store_dwordx4 v[132:135], v108, s[4:7], s8 offen offset:2048 sc1\n"
    "ds_read_b64 v[24:25], v15 offset:512\n"
    "s_lshl_b32 s55, s28, 12\n"
    "s_add_u32 s55, s55, 114688\n"
    "ds_read_b64 v[26:27], v15 offset:520\n"
    "v_bfe_u32 v88, v0, 4, 2\n"
    "v_and_b32 v89, 15, v0\n"
    "ds_read_b64 v[28:29], v15 offset:576\n"
    "v_lshrrev_b32 v7, 1, v0\n"
    "v_xor_b32 v7, v7, v88\n"
    "ds_read_b64 v[30:31], v15 offset:584\n"
    "v_and_b32 v7, 3, v7\n"
    "v_lshlrev_b32 v7, 4, v7\n"
    "ds_read_b64 v[32:33], v15 offset:1024\n"
    "v_lshl_add_u32 v7, v89, 6, v7\n"
    "v_add_u32 v7, s55, v7\n"
    "ds_read_b64 v[34:35], v15 offset:1032\n"
    "v_bfe_u32 v89, v0, 3, 1\n"
    "v_lshl_add_u32 v89, v89, 2, v88\n"
    "ds_read_b64 v[36:37], v15 offset:1088\n"
    "v_and_b32 v13, 7, v0\n"
    "v_lshlrev_b32 v13, 4, v13\n"
    "ds_read_b64 v[38:39], v15 offset:1096\n"
    "v_lshl_add_u32 v13, v89, 12, v13\n"
    "v_lshrrev_b32 v88, 3, v0\n"
    "ds_read_b64 v[40:41], v15 offset:1536\n"
    "v_and_b32 v89, 112, v1\n"
    "v_lshl_add_u32 v9, v88, 15, v89\n"
    "ds_read_b64 v[42:43], v15 offset:1544\n"
    "v_bfe_u32 v8, v0, 3, 2\n"
    "v_xor_b32 v8, v8, v0\n"
    "ds_read_b64 v[44:45], v15 offset:1600\n"
    "v_and_b32 v8, 0xff, v8\n"
    "v_lshlrev_b32 v8, 4, v8\n"
    "ds_read_b64 v[46:47], v15 offset:1608\n"
    "v_add_u32 v8, 114688, v8\n"
    "s_lshr_b32 s48, s30, 3\n"
    "ds_read_b64 v[48:49], v15 offset:2048\n"
    "s_mul_i32 s38, s48, 0\n"
    "s_mul_i32 s50, s35, 1\n"
    "ds_read_b64 v[50:51], v15 offset:2056\n"
    "s_add_u32 s38, s38, s50\n"
    "s_add_u32 s38, s38, s28\n"
    "ds_read_b64 v[52:53], v15 offset:2112\n"
    "s_and_b32 s38, s38, 7\n"
    "s_mul_i32 s50, s38, 229376\n"
    "ds_read_b64 v[54:55], v15 offset:2120\n"
    "s_lshl_b32 s51, s35, 7\n"
    "s_add_u32 s50, s50, s51\n"
    "ds_read_b64 v[56:57], v15 offset:2560\n"
    "v_add_u32 v13, s50, v13\n"
    "s_lshr_b32 s48, s30, 3\n"
    "ds_read_b64 v[58:59], v15 offset:2568\n"
    "s_add_u32 s34, s31, 2\n"
    "s_and_b32 s34, s34, 7\n"
    "ds_read_b64 v[60:61], v15 offset:2624\n"
    "s_lshl_b32 s34, s34, 2\n"
    "s_add_u32 s34, s34, s48\n"
    "ds_read_b64 v[62:63], v15 offset:2632\n"
    "s_and_b32 s43, s39, 0xf8\n"
    "s_and_b32 s49, s29, 1\n"
    "ds_read_b64 v[64:65], v15 offset:3072\n"
    "s_lshl_b32 s49, s49, 2\n"
    "s_add_u32 s43, s43, s49\n"
    "ds_read_b64 v[66:67], v15 offset:3080\n"
    "s_bfe_u32 s49, s29, 0x20001\n"
    "s_add_u32 s43, s43, s49\n"
    "ds_read_b64 v[68:69], v15 offset:3136\n"
    "s_lshl_b32 s43, s43, 7\n"
    "s_add_u32 s43, s43, 1048576\n"
    "ds_read_b64 v[70:71], v15 offset:3144\n"
    "v_lshl_add_u32 v11, s28, 11, v1\n"
    "s_xor_b32 s39, s48, s35\n"
    "ds_read_b64 v[72:73], v15 offset:3584\n"
    "s_and_b32 s39, s39, 1\n"
    "ds_read_b64 v[74:75], v15 offset:3592\n"
    "ds_read_b64 v[76:77], v15 offset:3648\n"
    "ds_read_b64 v[78:79], v15 offset:3656\n"
    "s_mov_b64 exec, 1\n"
    "ds_inc_u32 v2, v2 offset:20\n"
    "s_mov_b64 exec, -1\n"
    "s_and_b32 s50, s32, 3\n"
    "s_cmp_eq_u32 s50, 1\n"
    "s_cselect_b32 s8, 0xcfff3ffc, -1\n"
    "s_cselect_b32 s9, 0x00fff3ff, -1\n"
    "s_cmp_eq_u32 s50, 2\n"
    "s_cselect_b32 s8, 0xf3ffcfff, s8\n"
    "s_cselect_b32 s9, 0x003ffcff, s9\n"
    "s_and_b32 s9, s9, 0x00ffffff\n"
    "s_add_u32 s50, s47, 0\n"
    "s_and_b32 s50, s50, 3\n"
    "s_mul_i32 s13, s50, 3584\n"
    "s_add_u32 s13, s13, s61\n"
    "s_add_u32 s50, s50, s15\n"
    "s_and_b32 s50, s50, 31\n"
    "s_mul_i32 s12, s50, 57344\n"
    "s_add_u32 m0, s13, 0\n"
    "s_mov_b64 exec, s[8:9]\n"
    "buffer_load_dwordx4 v10, s[16:19], s12 offen lds\n"
    "s_add_u32 m0, s13, 896\n"
    "s_add_u32 s12, s12, 14336\n"
    "buffer_load_dwordx4 v10, s[16:19], s12 offen lds\n"
    "s_add_u32 m0, s13, 1792\n"
    "s_add_u32 s12, s12, 14336\n"
    "buffer_load_dwordx4 v10, s[16:19], s12 offen lds\n"
    "s_add_u32 m0, s13, 2688\n"
    "s_add_u32 s12, s12, 14336\n"
    "buffer_load_dwordx4 v10, s[16:19], s12 offen lds\n"
    "s_add_u32 s50, s47, 1\n"
    "s_and_b32 s50, s50, 3\n"
    "s_mul_i32 s13, s50, 3584\n"
    "s_add_u32 s13, s13, s61\n"
    "s_add_u32 s50, s50, s15\n"
    "s_and_b32 s50, s50, 31\n"
    "s_mul_i32 s12, s50, 57344\n"
    "s_add_u32 m0, s13, 0\n"
    "s_nop 0\n"
    "buffer_load_dwordx4 v10, s[16:19], s12 offen lds\n"
    "s_add_u32 m0, s13, 896\n"
    "s_add_u32 s12, s12, 14336\n"
    "buffer_load_dwordx4 v10, s[16:19], s12 offen lds\n"
    "s_add_u32 m0, s13, 1792\n"
    "s_add_u32 s12, s12, 14336\n"
    "buffer_load_dwordx4 v10, s[16:19], s12 offen lds\n"
    "s_add_u32 m0, s13, 2688\n"
    "s_add_u32 s12, s12, 14336\n"
    "buffer_load_dwordx4 v10, s[16:19], s12 offen lds\n"
    "s_add_u32 s50, s47, 2\n"
    "s_and_b32 s50, s50, 3\n"
    "s_mul_i32 s13, s50, 3584\n"
    "s_add_u32 s13, s13, s61\n"
    "s_add_u32 s50, s50, s15\n"
    "s_and_b32 s50, s50, 31\n"
    "s_mul_i32 s12, s50, 57344\n"
    "s_add_u32 m0, s13, 0\n"
    "s_nop 0\n"
    "buffer_load_dwordx4 v10, s[16:19], s12 offen lds\n"
    "s_add_u32 m0, s13, 896\n"
    "s_add_u32 s12, s12, 14336\n"
    "buffer_load_dwordx4 v10, s[16:19], s12 offen lds\n"
    "s_add_u32 m0, s13, 1792\n"
    "s_add_u32 s12, s12, 14336\n"
    "buffer_load_dwordx4 v10, s[16:19], s12 offen lds\n"
    "s_add_u32 m0, s13, 2688\n"
    "s_add_u32 s12, s12, 14336\n"
    "buffer_load_dwordx4 v10, s[16:19], s12 offen lds\n"
    "s_mov_b64 exec, -1\n"
    "s_mov_b32 s14, 16384\n"
    "s_cmp_eq_u32 s36, 1\n"
    "s_cbranch_scc1 .LconvL\n"
    "buffer_load_dwordx4 v[114:117], v1, s[4:7], s44 offen sc1\n"
    "buffer_load_dwordx2 v[118:119], v230, s[4:7], s44 offen offset:3072 sc1\n"
    "s_waitcnt vmcnt(0)\n"
    "v_xor_b32 v117, s37, v117\n"
    "v_xor_b32 v119, s37, v119\n"
    "v_or_b32 v117, v117, v119\n"
    "v_cmp_ne_u32_e32 vcc, 0, v117\n"
    "s_cbranch_vccnz .Lrec_s0\n"
    ".Lrec_b0:\n"
    "ds_write_b16 v12, v114 offset:24\n"
    "ds_write_b16_d16_hi v12, v114 offset:4696\n"
    "ds_write_b16 v12, v115 offset:16\n"
    "ds_write_b16_d16_hi v12, v115 offset:4688\n"
    "ds_write_b16 v12, v116 offset:8\n"
    "ds_write_b16_d16_hi v12, v116 offset:4680\n"
    "ds_write_b16 v12, v118 offset:0\n"
    "ds_write_b16_d16_hi v12, v118 offset:4672\n"
    "s_mov_b64 exec, 1\n"
    "ds_inc_u32 v14, v2 offset:24\n"
    "s_mov_b64 exec, -1\n"
    ".Lw_2:\n"
    "ds_read_b32 v3, v2 offset:24\n"
    "s_waitcnt lgkmcnt(0)\n"
    "v_readfirstlane_b32 s48, v3\n"
    "s_cmp_ge_u32 s48, 4\n"
    "s_cbranch_scc1 .Ld_1\n"
    "s_add_u32 s40, s40, 1\n"
    "s_cmp_lt_u32 s40, 262144\n"
    "s_cbranch_scc1 .Lw_2\n"
    ".Ld_1:\n"
    "ds_read_b128 v[112:115], v6 offset:2048\n"
    "ds_read_b128 v[116:119], v6 offset:2112\n"
    "ds_read_b128 v[120:123], v6 offset:2560\n"
    "ds_read_b128 v[124:127], v6 offset:2624\n"
    "ds_read_b128 v[88:91], v6 offset:3072\n"
    "ds_read_b128 v[92:95], v6 offset:3136\n"
    "ds_read_b128 v[80:83], v6 offset:3584\n"
    "ds_read_b128 v[84:87], v6 offset:3648\n"
    "ds_read_b32 v4, v2 offset:20\n"
    "s_waitcnt lgkmcnt(8)\n"
    "v_mfma_f32_16x16x32_bf16 v[108:111], v[16:19], v[112:115], 0\n"
    "buffer_load_dwordx4 v[128:131], v1, s[4:7], s44 offen offset:1024 sc1\n"
    "buffer_load_dwordx4 v[132:135], v1, s[4:7], s44 offen offset:2048 sc1\n"
    "buffer_load_dwordx4 v[136:139], v11, s[4:7], s14 offen sc1\n"
    "buffer_load_dwordx4 v[140:143], v11, s[4:7], s14 offen offset:1024 sc1\n"
    "s_lshl_b32 s50, %1, 3\n"
    "s_add_u32 s50, s50, s28\n"
    "s_mul_i32 s50, s50, 896\n"
    "v_lshlrev_b32 v254, 3, v1\n"
    "s_mov_b64 exec, 0x7f\n"
    "buffer_load_dword v254, v254, s[20:23], s50 offen\n"
    "s_mov_b64 exec, -1\n"
    "s_waitcnt lgkmcnt(7)\n"
    "v_mfma_f32_16x16x32_bf16 v[108:111], v[20:23], v[116:119], v[108:111]\n"
    "s_lshl_b32 s50, s30, 3\n"
    "s_add_u32 s50, s50, s28\n"
    "s_mul_i32 s50, s50, 7168\n"
    "v_lshlrev_b32 v253, 6, v1\n"
    "s_mov_b64 exec, 0x7f\n"
    "buffer_load_dword v253, v253, s[20:23], s50 offen offset:384\n"
    "s_mov_b64 exec, -1\n"
    "s_waitcnt lgkmcnt(6)\n"
    "v_mfma_f32_16x16x32_bf16 v[104:107], v[16:19], v[120:123], 0\n"
    "s_waitcnt lgkmcnt(5)\n"
    "v_mfma_f32_16x16x32_bf16 v[104:107], v[20:23], v[124:127], v[104:107]\n"
    "v_mfma_f32_16x16x32_bf16 v[108:111], v[24:27], v[120:123], v[108:111]\n"
    "v_mfma_f32_16x16x32_bf16 v[108:111], v[28:31], v[124:127], v[108:111]\n"
    "s_waitcnt lgkmcnt(4)\n"
    "v_mfma_f32_16x16x32_bf16 v[100:103], v[16:19], v[88:91], 0\n"
    "s_waitcnt lgkmcnt(3)\n"
    "v_mfma_f32_16x16x32_bf16 v[100:103], v[20:23], v[92:95], v[100:103]\n"
    "v_mfma_f32_16x16x32_bf16 v[104:107], v[24:27], v[88:91], v[104:107]\n"
    "s_waitcnt lgkmcnt(0)\n"
    "v_readfirstlane_b32 s48, v4\n"
    "s_cmp_ge_u32 s48, 8\n"
    "s_cbranch_scc0 .Ls_3\n"
    ".Lb_4:\n"
    "v_mfma_f32_16x16x32_bf16 v[104:107], v[28:31], v[92:95], v[104:107]\n"
    "s_add_u32 s50, s47, 3\n"
    "s_and_b32 s50, s50, 3\n"
    "s_mul_i32 s13, s50, 3584\n"
    "s_add_u32 s13, s13, s61\n"
    "s_add_u32 s50, s50, s15\n"
    "s_and_b32 s50, s50, 31\n"
    "s_mul_i32 s12, s50, 57344\n"
    "s_add_u32 m0, s13, 0\n"
    "s_mov_b64 exec, s[8:9]\n"
    "buffer_load_dwordx4 v10, s[16:19], s12 offen lds\n"
    "s_mov_b64 exec, -1\n"
    "v_mfma_f32_16x16x32_bf16 v[108:111], v[32:35], v[88:91], v[108:111]\n"
    "v_mfma_f32_16x16x32_bf16 v[108:111], v[36:39], v[92:95], v[108:111]\n"
    "v_mfma_f32_16x16x32_bf16 v[96:99], v[16:19], v[80:83], 0\n"
    "s_mul_i32 s45, s39, 114688\n"
    "buffer_load_dwordx4 v[144:147], v13, s[20:23], s45 offen offset:0\n"
    "v_mfma_f32_16x16x32_bf16 v[96:99], v[20:23], v[84:87], v[96:99]\n"
    "v_mfma_f32_16x16x32_bf16 v[100:103], v[24:27], v[80:83], v[100:103]\n"
    "v_mfma_f32_16x16x32_bf16 v[100:103], v[28:31], v[84:87], v[100:103]\n"
    "s_add_u32 m0, s13, 896\n"
    "s_add_u32 s12, s12, 14336\n"
    "s_mov_b64 exec, s[8:9]\n"
    "buffer_load_dwordx4 v10, s[16:19], s12 offen lds\n"
    "s_mov_b64 exec, -1\n"
    "v_mfma_f32_16x16x32_bf16 v[104:107], v[32:35], v[80:83], v[104:107]\n"
    "ds_write_b128 v7, v[96:99] offset:0\n"
    "v_mfma_f32_16x16x32_bf16 v[104:107], v[36:39], v[84:87], v[104:107]\n"
    "v_mfma_f32_16x16x32_bf16 v[108:111], v[40:43], v[80:83], v[108:111]\n"
    "ds_write_b128 v7, v[100:103] offset:1024\n"
    "v_mfma_f32_16x16x32_bf16 v[108:111], v[44:47], v[84:87], v[108:111]\n"
    "v_mfma_f32_16x16x32_bf16 v[124:127], v[72:75], v[80:83], 0\n"
    "ds_write_b128 v7, v[104:107] offset:2048\n"
    "buffer_load_dwordx4 v[148:151], v13, s[20:23], s45 offen offset:1024\n"
    "s_waitcnt vmcnt(6)\n"
    "v_xor_b32 v131, s37, v131\n"
    "v_xor_b32 v135, s37, v135\n"
    "v_or_b32 v12, v131, v135\n"
    "v_cmp_ne_u32_e32 vcc, 0, v12\n"
    "v_mfma_f32_16x16x32_bf16 v[124:127], v[76:79], v[84:87], v[124:127]\n"
    "s_add_u32 m0, s13, 1792\n"
    "s_add_u32 s12, s12, 14336\n"
    "s_mov_b64 exec, s[8:9]\n"
    "buffer_load_dwordx4 v10, s[16:19], s12 offen lds\n"
    "s_mov_b64 exec, -1\n"
    "s_mov_b64 s[10:11], vcc\n"
    "v_xor_b32 v139, s37, v139\n"
    "v_xor_b32 v143, s37, v143\n"
    "v_or_b32 v12, v139, v143\n"
    "v_max_u32 v138, v138, v142\n"
    "v_mfma_f32_16x16x32_bf16 v[120:123], v[64:67], v[80:83], 0\n"
    "ds_write_b128 v7, v[108:111] offset:3072\n"
    "s_mov_b64 exec, 1\n"
    "ds_inc_u32 v2, v2 offset:32\n"
    "s_mov_b64 exec, -1\n"
    "v_cmp_ne_u32_e32 vcc, 0, v12\n"
    "v_mfma_f32_16x16x32_bf16 v[120:123], v[68:71], v[84:87], v[120:123]\n"
    "s_or_b64 s[10:11], s[10:11], vcc\n"
    "s_cselect_b64 exec, 0, -1\n"
    "ds_max_u32 v2, v138 offset:4\n"
    "s_and_b64 exec, exec, 1\n"
    "ds_inc_u32 v2, v2 offset:16\n"
    "s_mov_b64 exec, -1\n"
    "v_mfma_f32_16x16x32_bf16 v[116:119], v[56:59], v[80:83], 0\n"
    "v_and_b32 v3, s60, v137\n"
    "v_rcp_f32 v3, v3\n"
    "v_lshlrev_b32 v12, 16, v137\n"
    "v_lshlrev_b32 v131, 16, v129\n"
    "v_mfma_f32_16x16x32_bf16 v[116:119], v[60:63], v[84:87], v[116:119]\n"
    "v_mul_f32 v12, v12, v3\n"
    "v_and_b32 v129, s60, v129\n"
    "v_fma_f32 v138, v131, v3, -v12\n"
    "v_fma_f32 v139, v129, v3, -v12\n"
    "v_mfma_f32_16x16x32_bf16 v[112:115], v[48:51], v[80:83], 0\n"
    "v_and_b32 v3, s60, v136\n"
    "v_rcp_f32 v3, v3\n"
    "v_lshlrev_b32 v12, 16, v136\n"
    "v_lshlrev_b32 v130, 16, v128\n"
    "v_mfma_f32_16x16x32_bf16 v[112:115], v[52:55], v[84:87], v[112:115]\n"
    "ds_read_b128 v[80:83], v6 offset:2560\n"
    "ds_read_b128 v[84:87], v6 offset:2624\n"
    "s_add_u32 m0, s13, 2688\n"
    "s_add_u32 s12, s12, 14336\n"
    "s_mov_b64 exec, s[8:9]\n"
    "buffer_load_dwordx4 v10, s[16:19], s12 offen lds\n"
    "s_mov_b64 exec, -1\n"
    "v_mul_f32 v12, v12, v3\n"
    "v_and_b32 v128, s60, v128\n"
    "v_fma_f32 v136, v130, v3, -v12\n"
    "v_fma_f32 v137, v128, v3, -v12\n"
    "ds_read_b32 v4, v2 offset:32\n"
    "v_mfma_f32_16x16x32_bf16 v[124:127], v[64:67], v[88:91], v[124:127]\n"
    "buffer_load_dwordx4 v[152:155], v13, s[20:23], s45 offen offset:2048\n"
    "v_and_b32 v3, s60, v141\n"
    "v_rcp_f32 v3, v3\n"
    "v_lshlrev_b32 v12, 16, v141\n"
    "v_lshlrev_b32 v135, 16, v133\n"
    "v_mfma_f32_16x16x32_bf16 v[124:127], v[68:71], v[92:95], v[124:127]\n"
    "v_mul_f32 v12, v12, v3\n"
    "v_and_b32 v133, s60, v133\n"
    "v_fma_f32 v142, v135, v3, -v12\n"
    "v_fma_f32 v143, v133, v3, -v12\n"
    "v_mfma_f32_16x16x32_bf16 v[120:123], v[56:59], v[88:91], v[120:123]\n"
    "s_waitcnt lgkmcnt(0)\n"
    "v_readfirstlane_b32 s48, v4\n"
    "s_cmp_ge_u32 s48, 8\n"
    "s_cbranch_scc0 .Ls_6\n"
    ".Lb_7:\n"
    "ds_read_b128 v[96:99], v8 offset:0\n"
    "ds_read_b128 v[100:103], v8 offset:4096\n"
    "ds_read_b128 v[104:107], v8 offset:8192\n"
    "ds_read_b128 v[108:111], v8 offset:12288\n"
    "ds_read_b128 v[72:75], v8 offset:16384\n"
    "ds_read_b128 v[76:79], v8 offset:20480\n"
    "ds_read_b128 v[64:67], v8 offset:24576\n"
    "ds_read_b128 v[68:71], v8 offset:28672\n"
    "s_mov_b64 exec, 1\n"
    "ds_inc_u32 v2, v2 offset:36\n"
    "s_mov_b64 exec, -1\n"
    "v_mfma_f32_16x16x32_bf16 v[120:123], v[60:63], v[92:95], v[120:123]\n"
    "v_and_b32 v3, s60, v140\n"
    "v_rcp_f32 v3, v3\n"
    "v_lshlrev_b32 v12, 16, v140\n"
    "v_lshlrev_b32 v134, 16, v132\n"
    "v_mfma_f32_16x16x32_bf16 v[116:119], v[48:51], v[88:91], v[116:119]\n"
    "v_mul_f32 v12, v12, v3\n"
    "v_and_b32 v132, s60, v132\n"
    "v_fma_f32 v140, v134, v3, -v12\n"
    "v_fma_f32 v141, v132, v3, -v12\n"
    "s_waitcnt lgkmcnt(7)\n"
    "v_add_f32 v96, v96, v100\n"
    "v_add_f32 v97, v97, v101\n"
    "v_mfma_f32_16x16x32_bf16 v[116:119], v[52:55], v[92:95], v[116:119]\n"
    "v_add_f32 v98, v98, v102\n"
    "v_add_f32 v99, v99, v103\n"
    "s_waitcnt lgkmcnt(5)\n"
    "v_mfma_f32_16x16x32_bf16 v[112:115], v[40:43], v[88:91], v[112:115]\n"
    "s_cmp_eq_u64 s[10:11], 0\n"
    "s_cbranch_scc1 .Lg_9\n"
    "s_call_b64 s[56:57], .LsubQ\n"
    ".Lg_9:\n"
    "v_add_f32 v104, v104, v108\n"
    "v_add_f32 v105, v105, v109\n"
    "v_add_f32 v106, v106, v110\n"
    "v_mfma_f32_16x16x32_bf16 v[112:115], v[44:47], v[92:95], v[112:115]\n"
    "ds_read_b128 v[88:91], v6 offset:2048\n"
    "ds_read_b128 v[92:95], v6 offset:2112\n"
    "v_add_f32 v107, v107, v111\n"
    "v_add_f32 v96, v96, v104\n"
    "v_add_f32 v97, v97, v105\n"
    "v_mfma_f32_16x16x32_bf16 v[124:127], v[56:59], v[80:83], v[124:127]\n"
    "buffer_load_dwordx4 v[156:159], v13, s[20:23], s45 offen offset:3072\n"
    "v_add_f32 v98, v98, v106\n"
    "v_add_f32 v99, v99, v107\n"
    "s_waitcnt lgkmcnt(5)\n"
    "v_mfma_f32_16x16x32_bf16 v[124:127], v[60:63], v[84:87], v[124:127]\n"
    "v_add_f32 v72, v72, v76\n"
    "v_add_f32 v73, v73, v77\n"
    "v_add_f32 v74, v74, v78\n"
    "v_mfma_f32_16x16x32_bf16 v[120:123], v[48:51], v[80:83], v[120:123]\n"
    "ds_read_b32 v5, v2 offset:28\n"
    "v_add_f32 v75, v75, v79\n"
    "s_waitcnt lgkmcnt(4)\n"
    "v_add_f32 v64, v64, v68\n"
    "v_mfma_f32_16x16x32_bf16 v[120:123], v[52:55], v[84:87], v[120:123]\n"
    "v_add_f32 v65, v65, v69\n"
    "v_add_f32 v66, v66, v70\n"
    "v_add_f32 v67, v67, v71\n"
    "v_mfma_f32_16x16x32_bf16 v[116:119], v[40:43], v[80:83], v[116:119]\n"
    "v_add_f32 v72, v72, v64\n"
    "v_add_f32 v73, v73, v65\n"
    "v_add_f32 v74, v74, v66\n"
    "v_mfma_f32_16x16x32_bf16 v[116:119], v[44:47], v[84:87], v[116:119]\n"
    "v_add_f32 v75, v75, v67\n"
    "ds_read_b32 v4, v2 offset:16\n"
    "v_fmac_f32 v130, v136, v96\n"
    "v_mfma_f32_16x16x32_bf16 v[112:115], v[32:35], v[80:83], v[112:115]\n"
    "v_fmac_f32 v131, v138, v97\n"
    "v_fmac_f32 v134, v140, v98\n"
    "v_fmac_f32 v135, v142, v99\n"
    "v_mfma_f32_16x16x32_bf16 v[112:115], v[36:39], v[84:87], v[112:115]\n"
    "s_waitcnt lgkmcnt(1)\n"
    "v_readfirstlane_b32 s48, v5\n"
    "s_cmp_ge_u32 s48, 4\n"
    "s_cbranch_scc0 .Ls_10\n"
    ".Lb_11:\n"
    "ds_read_b128 v[80:83], v6 offset:1536\n"
    "ds_read_b128 v[84:87], v6 offset:1600\n"
    "v_fmac_f32 v128, v137, v72\n"
    "v_fmac_f32 v129, v139, v73\n"
    "v_fmac_f32 v132, v141, v74\n"
    "v_mfma_f32_16x16x32_bf16 v[124:127], v[48:51], v[88:91], v[124:127]\n"
    "s_add_u32 s45, s45, 32768\n"
    "buffer_load_dwordx4 v[160:163], v13, s[20:23], s45 offen offset:0\n"
    "v_fmac_f32 v133, v143, v75\n"
    "v_cvt_pk_bf16_f32 v100, v130, v128\n"
    "v_cvt_pk_bf16_f32 v101, v131, v129\n"
    "v_mfma_f32_16x16x32_bf16 v[124:127], v[52:55], v[92:95], v[124:127]\n"
    "v_cvt_pk_bf16_f32 v102, v134, v132\n"
    "v_cvt_pk_bf16_f32 v103, v135, v133\n"
    "v_min_u32 v100, -2, v100\n"
    "v_mfma_f32_16x16x32_bf16 v[120:123], v[40:43], v[88:91], v[120:123]\n"
    "v_min_u32 v101, -2, v101\n"
    "v_min_u32 v102, -2, v102\n"
    "v_min_u32 v103, -2, v103\n"
    "v_mfma_f32_16x16x32_bf16 v[120:123], v[44:47], v[92:95], v[120:123]\n"
    "s_waitcnt lgkmcnt(2)\n"
    "v_readfirstlane_b32 s48, v4\n"
    "s_cmp_ge_u32 s48, 8\n"
    "s_cbranch_scc0 .Ls_13\n"
    ".Lb_14:\n"
    "buffer_store_dwordx4 v[100:103], v9, s[4:7], s43 offen sc1\n"
    "v_mfma_f32_16x16x32_bf16 v[116:119], v[32:35], v[88:91], v[116:119]\n"
    "v_mfma_f32_16x16x32_bf16 v[116:119], v[36:39], v[92:95], v[116:119]\n"
    "v_mfma_f32_16x16x32_bf16 v[112:115], v[24:27], v[88:91], v[112:115]\n"
    "ds_read_b32 v4, v2 offset:36\n"
    "v_mfma_f32_16x16x32_bf16 v[112:115], v[28:31], v[92:95], v[112:115]\n"
    "ds_read_b128 v[88:91], v6 offset:1024\n"
    "ds_read_b128 v[92:95], v6 offset:1088\n"
    "s_waitcnt lgkmcnt(4)\n"
    "v_mfma_f32_16x16x32_bf16 v[124:127], v[40:43], v[80:83], v[124:127]\n"
    "buffer_load_dwordx4 v[164:167], v13, s[20:23], s45 offen offset:1024\n"
    "s_waitcnt lgkmcnt(3)\n"
    "v_mfma_f32_16x16x32_bf16 v[124:127], v[44:47], v[84:87], v[124:127]\n"
    "ds_read_b128 v[56:59], v6 offset:512\n"
    "ds_read_b128 v[60:63], v6 offset:576\n"
    "v_mfma_f32_16x16x32_bf16 v[120:123], v[32:35], v[80:83], v[120:123]\n"
    "v_mfma_f32_16x16x32_bf16 v[120:123], v[36:39], v[84:87], v[120:123]\n"
    "ds_read_b128 v[48:51], v6 offset:0\n"
    "ds_read_b128 v[52:55], v6 offset:64\n"
    "v_mfma_f32_16x16x32_bf16 v[116:119], v[24:27], v[80:83], v[116:119]\n"
    "s_waitcnt lgkmcnt(6)\n"
    "v_readfirstlane_b32 s48, v4\n"
    "s_cmp_ge_u32 s48, 4\n"
    "s_cbranch_scc0 .Ls_16\n"
    ".Lb_17:\n"
    "v_mfma_f32_16x16x32_bf16 v[116:119], v[28:31], v[84:87], v[116:119]\n"
    "v_mfma_f32_16x16x32_bf16 v[112:115], v[16:19], v[80:83], v[112:115]\n"
    "v_mfma_f32_16x16x32_bf16 v[112:115], v[20:23], v[84:87], v[112:115]\n"
    "s_waitcnt lgkmcnt(5)\n"
    "v_mfma_f32_16x16x32_bf16 v[124:127], v[32:35], v[88:91], v[124:127]\n"
    "buffer_load_dwordx4 v[168:171], v13, s[20:23], s45 offen offset:2048\n"
    "s_waitcnt lgkmcnt(4)\n"
    "v_mfma_f32_16x16x32_bf16 v[124:127], v[36:39], v[92:95], v[124:127]\n"
    "v_mfma_f32_16x16x32_bf16 v[120:123], v[24:27], v[88:91], v[120:123]\n"
    "ds_write_b128 v7, v[112:115] offset:0\n"
    "v_mfma_f32_16x16x32_bf16 v[120:123], v[28:31], v[92:95], v[120:123]\n"
    "v_mfma_f32_16x16x32_bf16 v[116:119], v[16:19], v[88:91], v[116:119]\n"
    "v_mfma_f32_16x16x32_bf16 v[116:119], v[20:23], v[92:95], v[116:119]\n"
    "s_waitcnt lgkmcnt(4)\n"
    "v_mfma_f32_16x16x32_bf16 v[124:127], v[24:27], v[56:59], v[124:127]\n"
    "s_waitcnt lgkmcnt(3)\n"
    "v_mfma_f32_16x16x32_bf16 v[124:127], v[28:31], v[60:63], v[124:127]\n"
    "v_mfma_f32_16x16x32_bf16 v[120:123], v[16:19], v[56:59], v[120:123]\n"
    "ds_write_b128 v7, v[116:119] offset:1024\n"
    "buffer_load_dwordx4 v[172:175], v13, s[20:23], s45 offen offset:3072\n"
    "v_mfma_f32_16x16x32_bf16 v[120:123], v[20:23], v[60:63], v[120:123]\n"
    "s_waitcnt lgkmcnt(3)\n"
    "v_mfma_f32_16x16x32_bf16 v[124:127], v[16:19], v[48:51], v[124:127]\n"
    "s_waitcnt lgkmcnt(2)\n"
    "v_mfma_f32_16x16x32_bf16 v[124:127], v[20:23], v[52:55], v[124:127]\n"
    "s_nop 3\n"
    "ds_write_b128 v7, v[120:123] offset:2048\n"
    "s_nop 2\n"
    "ds_write_b128 v7, v[124:127] offset:3072\n"
    "s_mov_b64 exec, 1\n"
    "ds_inc_u32 v2, v2 offset:44\n"
    "s_mov_b64 exec, -1\n"
    "s_add_u32 s50, s32, 1\n"
    "s_bitcmp1_b32 s50, 1\n"
    "s_cbranch_scc0 .Lot2\n"
    "s_lshr_b32 s50, s32, 1\n"
    "s_lshl_b32 s50, s50, 14\n"
    "s_add_u32 s50, s50, 524288\n"
    "s_lshl_b32 s51, s28, 2\n"
    "s_add_u32 s51, s51, s32\n"
    "s_add_u32 s49, s51, 0\n"
    "s_and_b32 s49, s49, 15\n"
    "s_lshl_b32 s49, s49, 10\n"
    "s_add_u32 s49, s49, s50\n"
    "buffer_load_dwordx4 v[232:235], v1, s[4:7], s49 offen sc1\n"
    "s_add_u32 s49, s51, 1\n"
    "s_and_b32 s49, s49, 15\n"
    "s_lshl_b32 s49, s49, 10\n"
    "s_add_u32 s49, s49, s50\n"
    "buffer_load_dwordx4 v[236:239], v1, s[4:7], s49 offen sc1\n"
    "s_add_u32 s49, s51, 2\n"
    "s_and_b32 s49, s49, 15\n"
    "s_lshl_b32 s49, s49, 10\n"
    "s_add_u32 s49, s49, s50\n"
    "buffer_load_dwordx4 v[240:243], v1, s[4:7], s49 offen sc1\n"
    "s_add_u32 s49, s51, 3\n"
    "s_and_b32 s49, s49, 15\n"
    "s_lshl_b32 s49, s49, 10\n"
    "s_add_u32 s49, s49, s50\n"
    "buffer_load_dwordx4 v[244:247], v1, s[4:7], s49 offen sc1\n"
    ".Lot2:\n"
    "s_branch .Lcsum\n"
    ".LconvL:\n"
    "buffer_load_dwordx4 v[224:227], v1, s[4:7], s44 offen sc1\n"
    "buffer_load_dwordx2 v[228:229], v230, s[4:7], s44 offen offset:3072 sc1\n"
    "buffer_load_dwordx4 v[128:131], v1, s[4:7], s44 offen offset:1024 sc1\n"
    "buffer_load_dwordx4 v[132:135], v1, s[4:7], s44 offen offset:2048 sc1\n"
    "buffer_load_dwordx4 v[136:139], v11, s[4:7], s14 offen sc1\n"
    "buffer_load_dwordx4 v[140:143], v11, s[4:7], s14 offen offset:1024 sc1\n"
    ".Lw_20:\n"
    "ds_read_b32 v3, v2 offset:24\n"
    "s_waitcnt lgkmcnt(0)\n"
    "v_readfirstlane_b32 s48, v3\n"
    "s_cmp_ge_u32 s48, 4\n"
    "s_cbranch_scc1 .Ld_19\n"
    "s_add_u32 s40, s40, 1\n"
    "s_cmp_lt_u32 s40, 262144\n"
    "s_cbranch_scc1 .Lw_20\n"
    ".Ld_19:\n"
    "ds_read_b128 v[112:115], v6 offset:2048\n"
    "ds_read_b128 v[116:119], v6 offset:2112\n"
    "ds_read_b128 v[120:123], v6 offset:2560\n"
    "ds_read_b128 v[124:127], v6 offset:2624\n"
    "ds_read_b128 v[88:91], v6 offset:3072\n"
    "ds_read_b128 v[92:95], v6 offset:3136\n"
    "ds_read_b128 v[80:83], v6 offset:3584\n"
    "ds_read_b128 v[84:87], v6 offset:3648\n"
    "ds_read_b32 v4, v2 offset:20\n"
    "s_waitcnt lgkmcnt(8)\n"
    "v_mfma_f32_16x16x32_bf16 v[108:111], v[16:19], v[112:115], 0\n"
    "s_waitcnt lgkmcnt(7)\n"
    "v_mfma_f32_16x16x32_bf16 v[108:111], v[20:23], v[116:119], v[108:111]\n"
    "s_waitcnt lgkmcnt(6)\n"
    "v_mfma_f32_16x16x32_bf16 v[104:107], v[16:19], v[120:123], 0\n"
    "s_waitcnt lgkmcnt(5)\n"
    "v_mfma_f32_16x16x32_bf16 v[104:107], v[20:23], v[124:127], v[104:107]\n"
    "v_mfma_f32_16x16x32_bf16 v[108:111], v[24:27], v[120:123], v[108:111]\n"
    "s_lshl_b32 s50, %1, 3\n"
    "s_add_u32 s50, s50, s28\n"
    "s_mul_i32 s50, s50, 896\n"
    "v_lshlrev_b32 v254, 3, v1\n"
    "s_mov_b64 exec, 0x7f\n"
    "buffer_load_dword v254, v254, s[20:23], s50 offen\n"
    "s_mov_b64 exec, -1\n"
    "v_mfma_f32_16x16x32_bf16 v[108:111], v[28:31], v[124:127], v[108:111]\n"
    "s_lshl_b32 s50, s30, 3\n"
    "s_add_u32 s50, s50, s28\n"
    "s_mul_i32 s50, s50, 7168\n"
    "v_lshlrev_b32 v253, 6, v1\n"
    "s_mov_b64 exec, 0x7f\n"
    "buffer_load_dword v253, v253, s[20:23], s50 offen offset:384\n"
    "s_mov_b64 exec, -1\n"
    "s_waitcnt lgkmcnt(4)\n"
    "v_mfma_f32_16x16x32_bf16 v[100:103], v[16:19], v[88:91], 0\n"
    "s_waitcnt lgkmcnt(3)\n"
    "v_mfma_f32_16x16x32_bf16 v[100:103], v[20:23], v[92:95], v[100:103]\n"
    "v_mfma_f32_16x16x32_bf16 v[104:107], v[24:27], v[88:91], v[104:107]\n"
    "s_waitcnt lgkmcnt(0)\n"
    "v_readfirstlane_b32 s48, v4\n"
    "s_cmp_ge_u32 s48, 8\n"
    "s_cbranch_scc0 .Ls_21\n"
    ".Lb_22:\n"
    "v_mfma_f32_16x16x32_bf16 v[104:107], v[28:31], v[92:95], v[104:107]\n"
    "v_mfma_f32_16x16x32_bf16 v[108:111], v[32:35], v[88:91], v[108:111]\n"
    "v_mfma_f32_16x16x32_bf16 v[108:111], v[36:39], v[92:95], v[108:111]\n"
    "v_mfma_f32_16x16x32_bf16 v[96:99], v[16:19], v[80:83], 0\n"
    "v_mfma_f32_16x16x32_bf16 v[96:99], v[20:23], v[84:87], v[96:99]\n"
    "v_mfma_f32_16x16x32_bf16 v[100:103], v[24:27], v[80:83], v[100:103]\n"
    "v_mfma_f32_16x16x32_bf16 v[100:103], v[28:31], v[84:87], v[100:103]\n"
    "v_mfma_f32_16x16x32_bf16 v[104:107], v[32:35], v[80:83], v[104:107]\n"
    "ds_write_b128 v7, v[96:99] offset:0\n"
    "v_mfma_f32_16x16x32_bf16 v[104:107], v[36:39], v[84:87], v[104:107]\n"
    "v_mfma_f32_16x16x32_bf16 v[108:111], v[40:43], v[80:83], v[108:111]\n"
    "ds_write_b128 v7, v[100:103] offset:1024\n"
    "v_mfma_f32_16x16x32_bf16 v[108:111], v[44:47], v[84:87], v[108:111]\n"
    "v_mfma_f32_16x16x32_bf16 v[124:127], v[72:75], v[80:83], 0\n"
    "ds_write_b128 v7, v[104:107] offset:2048\n"
    "v_mfma_f32_16x16x32_bf16 v[124:127], v[76:79], v[84:87], v[124:127]\n"
    "v_mfma_f32_16x16x32_bf16 v[120:123], v[64:67], v[80:83], 0\n"
    "ds_write_b128 v7, v[108:111] offset:3072\n"
    "s_mov_b64 exec, 1\n"
    "ds_inc_u32 v2, v2 offset:32\n"
    "s_mov_b64 exec, -1\n"
    "v_mfma_f32_16x16x32_bf16 v[120:123], v[68:71], v[84:87], v[120:123]\n"
    "s_waitcnt vmcnt(6)\n"
    "v_xor_b32 v227, s37, v227\n"
    "v_xor_b32 v229, s37, v229\n"
    "v_or_b32 v227, v227, v229\n"
    "v_cmp_ne_u32_e32 vcc, 0, v227\n"
    "s_cbranch_vccnz .Lrec_s1\n"
    ".Lrec_b1:\n"
    "ds_write_b16 v12, v224 offset:24\n"
    "ds_write_b16_d16_hi v12, v224 offset:4696\n"
    "ds_write_b16 v12, v225 offset:16\n"
    "ds_write_b16_d16_hi v12, v225 offset:4688\n"
    "ds_write_b16 v12, v226 offset:8\n"
    "ds_write_b16_d16_hi v12, v226 offset:4680\n"
    "ds_write_b16 v12, v228 offset:0\n"
    "ds_write_b16_d16_hi v12, v228 offset:4672\n"
    "s_mov_b64 exec, 1\n"
    "ds_inc_u32 v14, v2 offset:24\n"
    "s_mov_b64 exec, -1\n"
    "v_mfma_f32_16x16x32_bf16 v[116:119], v[56:59], v[80:83], 0\n"
    "s_add_u32 s50, s47, 3\n"
    "s_and_b32 s50, s50, 3\n"
    "s_mul_i32 s13, s50, 3584\n"
    "s_add_u32 s13, s13, s61\n"
    "s_add_u32 s50, s50, s15\n"
    "s_and_b32 s50, s50, 31\n"
    "s_mul_i32 s12, s50, 57344\n"
    "s_add_u32 m0, s13, 0\n"
    "s_mov_b64 exec, s[8:9]\n"
    "buffer_load_dwordx4 v10, s[16:19], s12 offen lds\n"
    "s_mov_b64 exec, -1\n"
    "v_mfma_f32_16x16x32_bf16 v[116:119], v[60:63], v[84:87], v[116:119]\n"
    "v_mfma_f32_16x16x32_bf16 v[112:115], v[48:51], v[80:83], 0\n"
    "s_waitcnt vmcnt(3)\n"
    "v_xor_b32 v131, s37, v131\n"
    "v_xor_b32 v135, s37, v135\n"
    "v_or_b32 v12, v131, v135\n"
    "v_cmp_ne_u32_e32 vcc, 0, v12\n"
    "v_mfma_f32_16x16x32_bf16 v[112:115], v[52:55], v[84:87], v[112:115]\n"
    "ds_read_b128 v[80:83], v6 offset:2560\n"
    "ds_read_b128 v[84:87], v6 offset:2624\n"
    "s_add_u32 m0, s13, 896\n"
    "s_add_u32 s12, s12, 14336\n"
    "s_mov_b64 exec, s[8:9]\n"
    "buffer_load_dwordx4 v10, s[16:19], s12 offen lds\n"
    "s_mov_b64 exec, -1\n"
    "s_mov_b64 s[10:11], vcc\n"
    "v_xor_b32 v139, s37, v139\n"
    "v_xor_b32 v143, s37, v143\n"
    "v_or_b32 v12, v139, v143\n"
    "v_max_u32 v138, v138, v142\n"
    "v_mfma_f32_16x16x32_bf16 v[124:127], v[64:67], v[88:91], v[124:127]\n"
    "v_cmp_ne_u32_e32 vcc, 0, v12\n"
    "v_mfma_f32_16x16x32_bf16 v[124:127], v[68:71], v[92:95], v[124:127]\n"
    "s_or_b64 s[10:11], s[10:11], vcc\n"
    "s_cselect_b64 exec, 0, -1\n"
    "ds_max_u32 v2, v138 offset:4\n"
    "s_and_b64 exec, exec, 1\n"
    "ds_inc_u32 v2, v2 offset:16\n"
    "s_mov_b64 exec, -1\n"
    "v_mfma_f32_16x16x32_bf16 v[120:123], v[56:59], v[88:91], v[120:123]\n"
    "s_add_u32 m0, s13, 1792\n"
    "s_add_u32 s12, s12, 14336\n"
    "s_mov_b64 exec, s[8:9]\n"
    "buffer_load_dwordx4 v10, s[16:19], s12 offen lds\n"
    "s_mov_b64 exec, -1\n"
    "s_mul_i32 s45, s39, 114688\n"
    "buffer_load_dwordx4 v[144:147], v13, s[20:23], s45 offen offset:0\n"
    "v_mfma_f32_16x16x32_bf16 v[120:123], v[60:63], v[92:95], v[120:123]\n"
    "v_and_b32 v3, s60, v137\n"
    "v_rcp_f32 v3, v3\n"
    "v_lshlrev_b32 v12, 16, v137\n"
    "v_lshlrev_b32 v131, 16, v129\n"
    "v_mfma_f32_16x16x32_bf16 v[116:119], v[48:51], v[88:91], v[116:119]\n"
    "buffer_load_dwordx4 v[148:151], v13, s[20:23], s45 offen offset:1024\n"
    "v_mul_f32 v12, v12, v3\n"
    "v_and_b32 v129, s60, v129\n"
    "v_fma_f32 v138, v131, v3, -v12\n"
    "v_fma_f32 v139, v129, v3, -v12\n"
    "v_mfma_f32_16x16x32_bf16 v[116:119], v[52:55], v[92:95], v[116:119]\n"
    "s_add_u32 m0, s13, 2688\n"
    "s_add_u32 s12, s12, 14336\n"
    "s_mov_b64 exec, s[8:9]\n"
    "buffer_load_dwordx4 v10, s[16:19], s12 offen lds\n"
    "s_mov_b64 exec, -1\n"
    "v_and_b32 v3, s60, v136\n"
    "v_rcp_f32 v3, v3\n"
    "v_lshlrev_b32 v12, 16, v136\n"
    "v_lshlrev_b32 v130, 16, v128\n"
    "v_mfma_f32_16x16x32_bf16 v[112:115], v[40:43], v[88:91], v[112:115]\n"
    "buffer_load_dwordx4 v[152:155], v13, s[20:23], s45 offen offset:2048\n"
    "v_mul_f32 v12, v12, v3\n"
    "v_and_b32 v128, s60, v128\n"
    "v_fma_f32 v136, v130, v3, -v12\n"
    "v_fma_f32 v137, v128, v3, -v12\n"
    "v_mfma_f32_16x16x32_bf16 v[112:115], v[44:47], v[92:95], v[112:115]\n"
    "ds_read_b128 v[88:91], v6 offset:2048\n"
    "ds_read_b128 v[92:95], v6 offset:2112\n"
    "v_and_b32 v3, s60, v141\n"
    "v_rcp_f32 v3, v3\n"
    "v_lshlrev_b32 v12, 16, v141\n"
    "v_lshlrev_b32 v135, 16, v133\n"
    "s_waitcnt lgkmcnt(3)\n"
    "v_mfma_f32_16x16x32_bf16 v[124:127], v[56:59], v[80:83], v[124:127]\n"
    "buffer_load_dwordx4 v[156:159], v13, s[20:23], s45 offen offset:3072\n"
    "v_mul_f32 v12, v12, v3\n"
    "v_and_b32 v133, s60, v133\n"
    "v_fma_f32 v142, v135, v3, -v12\n"
    "v_fma_f32 v143, v133, v3, -v12\n"
    "s_waitcnt lgkmcnt(2)\n"
    "v_mfma_f32_16x16x32_bf16 v[124:127], v[60:63], v[84:87], v[124:127]\n"
    "v_and_b32 v3, s60, v140\n"
    "v_rcp_f32 v3, v3\n"
    "v_lshlrev_b32 v12, 16, v140\n"
    "v_lshlrev_b32 v134, 16, v132\n"
    "v_mfma_f32_16x16x32_bf16 v[120:123], v[48:51], v[80:83], v[120:123]\n"
    "ds_read_b32 v5, v2 offset:28\n"
    "s_add_u32 s45, s45, 32768\n"
    "buffer_load_dwordx4 v[160:163], v13, s[20:23], s45 offen offset:0\n"
    "v_mul_f32 v12, v12, v3\n"
    "v_and_b32 v132, s60, v132\n"
    "v_fma_f32 v140, v134, v3, -v12\n"
    "v_fma_f32 v141, v132, v3, -v12\n"
    "v_mfma_f32_16x16x32_bf16 v[120:123], v[52:55], v[84:87], v[120:123]\n"
    "v_mfma_f32_16x16x32_bf16 v[116:119], v[40:43], v[80:83], v[116:119]\n"
    "buffer_load_dwordx4 v[164:167], v13, s[20:23], s45 offen offset:1024\n"
    "v_mfma_f32_16x16x32_bf16 v[116:119], v[44:47], v[84:87], v[116:119]\n"
    "v_mfma_f32_16x16x32_bf16 v[112:115], v[32:35], v[80:83], v[112:115]\n"
    "buffer_load_dwordx4 v[168:171], v13, s[20:23], s45 offen offset:2048\n"
    "v_mfma_f32_16x16x32_bf16 v[112:115], v[36:39], v[84:87], v[112:115]\n"
    "s_waitcnt lgkmcnt(0)\n"
    "v_readfirstlane_b32 s48, v5\n"
    "s_cmp_ge_u32 s48, 4\n"
    "s_cbranch_scc0 .Ls_24\n"
    ".Lb_25:\n"
    "ds_read_b128 v[80:83], v6 offset:1536\n"
    "ds_read_b128 v[84:87], v6 offset:1600\n"
    "v_mfma_f32_16x16x32_bf16 v[124:127], v[48:51], v[88:91], v[124:127]\n"
    "buffer_load_dwordx4 v[172:175], v13, s[20:23], s45 offen offset:3072\n"
    "s_cmp_eq_u64 s[10:11], 0\n"
    "s_cbranch_scc1 .Lg_27\n"
    "s_call_b64 s[56:57], .LsubQ\n"
    ".Lg_27:\n"
    "v_mfma_f32_16x16x32_bf16 v[124:127], v[52:55], v[92:95], v[124:127]\n"
    "v_mfma_f32_16x16x32_bf16 v[120:123], v[40:43], v[88:91], v[120:123]\n"
    "s_add_u32 s45, s45, 32768\n"
    "buffer_load_dwordx4 v[176:179], v13, s[20:23], s45 offen offset:0\n"
    "v_mfma_f32_16x16x32_bf16 v[120:123], v[44:47], v[92:95], v[120:123]\n"
    "v_mfma_f32_16x16x32_bf16 v[116:119], v[32:35], v[88:91], v[116:119]\n"
    "buffer_load_dwordx4 v[180:183], v13, s[20:23], s45 offen offset:1024\n"
    "v_mfma_f32_16x16x32_bf16 v[116:119], v[36:39], v[92:95], v[116:119]\n"
    "v_mfma_f32_16x16x32_bf16 v[112:115], v[24:27], v[88:91], v[112:115]\n"
    "ds_read_b32 v4, v2 offset:36\n"
    "buffer_load_dwordx4 v[184:187], v13, s[20:23], s45 offen offset:2048\n"
    "v_mfma_f32_16x16x32_bf16 v[112:115], v[28:31], v[92:95], v[112:115]\n"
    "ds_read_b128 v[88:91], v6 offset:1024\n"
    "ds_read_b128 v[92:95], v6 offset:1088\n"
    "s_waitcnt lgkmcnt(4)\n"
    "v_mfma_f32_16x16x32_bf16 v[124:127], v[40:43], v[80:83], v[124:127]\n"
    "buffer_load_dwordx4 v[188:191], v13, s[20:23], s45 offen offset:3072\n"
    "s_waitcnt lgkmcnt(3)\n"
    "v_mfma_f32_16x16x32_bf16 v[124:127], v[44:47], v[84:87], v[124:127]\n"
    "ds_read_b128 v[56:59], v6 offset:512\n"
    "ds_read_b128 v[60:63], v6 offset:576\n"
    "v_mfma_f32_16x16x32_bf16 v[120:123], v[32:35], v[80:83], v[120:123]\n"
    "s_xor_b32 s45, s39, 1\n"
    "s_mul_i32 s45, s45, 114688\n"
    "buffer_load_dwordx4 v[192:195], v13, s[20:23], s45 offen offset:0\n"
    "v_mfma_f32_16x16x32_bf16 v[120:123], v[36:39], v[84:87], v[120:123]\n"
    "ds_read_b128 v[48:51], v6 offset:0\n"
    "ds_read_b128 v[52:55], v6 offset:64\n"
    "v_mfma_f32_16x16x32_bf16 v[116:119], v[24:27], v[80:83], v[116:119]\n"
    "s_waitcnt lgkmcnt(6)\n"
    "v_readfirstlane_b32 s48, v4\n"
    "s_cmp_ge_u32 s48, 4\n"
    "s_cbranch_scc0 .Ls_28\n"
    ".Lb_29:\n"
    "buffer_load_dwordx4 v[196:199], v13, s[20:23], s45 offen offset:1024\n"
    "v_mfma_f32_16x16x32_bf16 v[116:119], v[28:31], v[84:87], v[116:119]\n"
    "v_mfma_f32_16x16x32_bf16 v[112:115], v[16:19], v[80:83], v[112:115]\n"
    "buffer_load_dwordx4 v[200:203], v13, s[20:23], s45 offen offset:2048\n"
    "v_mfma_f32_16x16x32_bf16 v[112:115], v[20:23], v[84:87], v[112:115]\n"
    "s_waitcnt lgkmcnt(5)\n"
    "v_mfma_f32_16x16x32_bf16 v[124:127], v[32:35], v[88:91], v[124:127]\n"
    "buffer_load_dwordx4 v[204:207], v13, s[20:23], s45 offen offset:3072\n"
    "s_waitcnt lgkmcnt(4)\n"
    "v_mfma_f32_16x16x32_bf16 v[124:127], v[36:39], v[92:95], v[124:127]\n"
    "v_mfma_f32_16x16x32_bf16 v[120:123], v[24:27], v[88:91], v[120:123]\n"
    "ds_write_b128 v7, v[112:115] offset:0\n"
    "s_add_u32 s45, s45, 32768\n"
    "buffer_load_dwordx4 v[208:211], v13, s[20:23], s45 offen offset:0\n"
    "v_mfma_f32_16x16x32_bf16 v[120:123], v[28:31], v[92:95], v[120:123]\n"
    "v_mfma_f32_16x16x32_bf16 v[116:119], v[16:19], v[88:91], v[116:119]\n"
    "buffer_load_dwordx4 v[212:215], v13, s[20:23], s45 offen offset:1024\n"
    "v_mfma_f32_16x16x32_bf16 v[116:119], v[20:23], v[92:95], v[116:119]\n"
    "s_waitcnt lgkmcnt(4)\n"
    "v_mfma_f32_16x16x32_bf16 v[124:127], v[24:27], v[56:59], v[124:127]\n"
    "buffer_load_dwordx4 v[216:219], v13, s[20:23], s45 offen offset:2048\n"
    "s_waitcnt lgkmcnt(3)\n"
    "v_mfma_f32_16x16x32_bf16 v[124:127], v[28:31], v[60:63], v[124:127]\n"
    "v_mfma_f32_16x16x32_bf16 v[120:123], v[16:19], v[56:59], v[120:123]\n"
    "ds_write_b128 v7, v[116:119] offset:1024\n"
    "buffer_load_dwordx4 v[220:223], v13, s[20:23], s45 offen offset:3072\n"
    "v_mfma_f32_16x16x32_bf16 v[120:123], v[20:23], v[60:63], v[120:123]\n"
    "s_waitcnt lgkmcnt(3)\n"
    "v_mfma_f32_16x16x32_bf16 v[124:127], v[16:19], v[48:51], v[124:127]\n"
    "s_waitcnt lgkmcnt(2)\n"
    "v_mfma_f32_16x16x32_bf16 v[124:127], v[20:23], v[52:55], v[124:127]\n"
    "s_nop 3\n"
    "ds_write_b128 v7, v[120:123] offset:2048\n"
    "s_nop 2\n"
    "ds_write_b128 v7, v[124:127] offset:3072\n"
    "s_mov_b64 exec, 1\n"
    "ds_inc_u32 v2, v2 offset:44\n"
    "s_mov_b64 exec, -1\n"
    ".Lw_32:\n"
    "ds_read_b32 v3, v2 offset:44\n"
    "s_waitcnt lgkmcnt(0)\n"
    "v_readfirstlane_b32 s48, v3\n"
    "s_cmp_ge_u32 s48, 8\n"
    "s_cbranch_scc1 .Ld_31\n"
    "s_add_u32 s40, s40, 1\n"
    "s_cmp_lt_u32 s40, 262144\n"
    "s_cbranch_scc1 .Lw_32\n"
    ".Ld_31:\n"
    "ds_read_b128 v[96:99], v8 offset:0\n"
    "ds_read_b128 v[100:103], v8 offset:4096\n"
    "ds_read_b128 v[104:107], v8 offset:8192\n"
    "ds_read_b128 v[108:111], v8 offset:12288\n"
    "ds_read_b128 v[72:75], v8 offset:16384\n"
    "ds_read_b128 v[76:79], v8 offset:20480\n"
    "ds_read_b128 v[64:67], v8 offset:24576\n"
    "ds_read_b128 v[68:71], v8 offset:28672\n"
    "s_mov_b64 exec, 1\n"
    "ds_inc_u32 v2, v2 offset:36\n"
    "s_mov_b64 exec, -1\n"
    "s_waitcnt lgkmcnt(7)\n"
    "v_add_f32 v96, v96, v100\n"
    "v_add_f32 v97, v97, v101\n"
    "v_add_f32 v98, v98, v102\n"
    "v_add_f32 v99, v99, v103\n"
    "s_waitcnt lgkmcnt(5)\n"
    "v_add_f32 v104, v104, v108\n"
    "v_add_f32 v105, v105, v109\n"
    "v_add_f32 v106, v106, v110\n"
    "v_add_f32 v107, v107, v111\n"
    "v_add_f32 v96, v96, v104\n"
    "v_add_f32 v97, v97, v105\n"
    "v_add_f32 v98, v98, v106\n"
    "v_add_f32 v99, v99, v107\n"
    "s_waitcnt lgkmcnt(3)\n"
    "v_add_f32 v72, v72, v76\n"
    "v_add_f32 v73, v73, v77\n"
    "v_add_f32 v74, v74, v78\n"
    "v_add_f32 v75, v75, v79\n"
    "s_waitcnt lgkmcnt(1)\n"
    "v_add_f32 v64, v64, v68\n"
    "v_add_f32 v65, v65, v69\n"
    "v_add_f32 v66, v66, v70\n"
    "v_add_f32 v67, v67, v71\n"
    "v_add_f32 v72, v72, v64\n"
    "v_add_f32 v73, v73, v65\n"
    "v_add_f32 v74, v74, v66\n"
    "v_add_f32 v75, v75, v67\n"
    "ds_read_b32 v4, v2 offset:16\n"
    "v_fmac_f32 v130, v136, v96\n"
    "v_fmac_f32 v131, v138, v97\n"
    "v_fmac_f32 v134, v140, v98\n"
    "v_fmac_f32 v135, v142, v99\n"
    "v_fmac_f32 v128, v137, v72\n"
    "v_fmac_f32 v129, v139, v73\n"
    "v_fmac_f32 v132, v141, v74\n"
    "v_fmac_f32 v133, v143, v75\n"
    "v_cvt_pk_bf16_f32 v100, v130, v128\n"
    "v_cvt_pk_bf16_f32 v101, v131, v129\n"
    "v_cvt_pk_bf16_f32 v102, v134, v132\n"
    "v_cvt_pk_bf16_f32 v103, v135, v133\n"
    "v_min_u32 v100, -2, v100\n"
    "v_min_u32 v101, -2, v101\n"
    "v_min_u32 v102, -2, v102\n"
    "v_min_u32 v103, -2, v103\n"
    "s_waitcnt lgkmcnt(0)\n"
    "v_readfirstlane_b32 s48, v4\n"
    "s_cmp_ge_u32 s48, 8\n"
    "s_cbranch_scc0 .Ls_33\n"
    ".Lb_34:\n"
    "buffer_store_dwordx4 v[100:103], v9, s[4:7], s43 offen sc1\n"
    "s_mov_b64 exec, 1\n"
    "ds_inc_u32 v2, v2 offset:96\n"
    "s_mov_b64 exec, -1\n"
    "s_waitcnt vmcnt(19)\n"
    "s_mov_b64 exec, 1\n"
    "ds_inc_u32 v2, v2 offset:48\n"
    "s_mov_b64 exec, -1\n"
    ".Lcsum:\n"
    "v_bfe_u32 v6, v0, 2, 4\n"
    "v_mul_u32_u24 v6, 224, v6\n"
    "v_and_b32 v3, 3, v0\n"
    "v_lshl_add_u32 v6, v3, 3, v6\n"
    "s_lshl_b32 s49, s28, 5\n"
    "v_add_u32 v7, s49, v6\n"
    "v_add_u32 v8, 57344, v7\n"
    "v_mov_b32 v48, 0x3f803f80\n"
    "v_mov_b32 v49, 0x3f803f80\n"
    "v_mov_b32 v50, 0x3f803f80\n"
    "v_mov_b32 v51, 0x3f803f80\n"
    "ds_read_b32 v5, v2 offset:4\n"
    "s_and_b32 s55, s28, 1\n"
    "s_and_b32 s53, s33, -2\n"
    "s_lshl_b32 s53, s53, 14\n"
    "s_add_u32 s53, s53, 1048576\n"
    "s_lshl_b32 s54, s55, 3\n"
    "s_add_u32 s54, s54, s32\n"
    "v_lshrrev_b32 v9, 1, v1\n"
    "v_lshl_add_u32 v9, s55, 9, v9\n"
    "v_xor_b32 v11, 512, v9\n"
    "s_cmp_eq_u32 s55, 0\n"
    "s_cselect_b64 s[16:17], -1, 0\n"
    "s_cselect_b64 s[18:19], 0, -1\n"
    "s_add_u32 s51, s54, 0\n"
    "s_and_b32 s51, s51, 15\n"
    "s_lshl_b32 s51, s51, 11\n"
    "s_add_u32 s8, s51, s53\n"
    "s_add_u32 s51, s54, 1\n"
    "s_and_b32 s51, s51, 15\n"
    "s_lshl_b32 s51, s51, 11\n"
    "s_add_u32 s9, s51, s53\n"
    "s_add_u32 s51, s54, 2\n"
    "s_and_b32 s51, s51, 15\n"
    "s_lshl_b32 s51, s51, 11\n"
    "s_add_u32 s10, s51, s53\n"
    "s_add_u32 s51, s54, 3\n"
    "s_and_b32 s51, s51, 15\n"
    "s_lshl_b32 s51, s51, 11\n"
    "s_add_u32 s11, s51, s53\n"
    "s_cmp_eq_u32 s36, 1\n"
    "s_cbranch_scc1 .Lh1\n"
    "s_waitcnt vmcnt(4)\n"
    "s_call_b64 s[56:57], .Lshuf0\n"
    "s_waitcnt vmcnt(0)\n"
    "s_call_b64 s[56:57], .Lshuf1\n"
    "s_add_u32 s50, s32, 1\n"
    "s_bitcmp1_b32 s50, 1\n"
    "s_cbranch_scc0 .Lot3\n"
    "s_and_b32 s50, s32, 2\n"
    "s_mul_i32 s50, s50, 96\n"
    "s_mul_i32 s51, s28, 28672\n"
    "s_add_u32 s50, s50, s51\n"
    "v_bfe_u32 v3, v0, 1, 5\n"
    "v_mul_u32_u24 v3, 192, v3\n"
    "v_add3_u32 v3, v3, v1, s50\n"
    "s_waitcnt vmcnt(0)\n"
    "ds_write_b128 v3, v[232:235] offset:0\n"
    "ds_write_b128 v3, v[236:239] offset:7168\n"
    "ds_write_b128 v3, v[240:243] offset:14336\n"
    "ds_write_b128 v3, v[244:247] offset:21504\n"
    ".Lot3:\n"
    "s_waitcnt vmcnt(0)\n"
    "s_mov_b64 exec, 1\n"
    "ds_inc_u32 v2, v2 offset:48\n"
    "s_mov_b64 exec, -1\n"
    "s_setprio 3\n"
    "s_waitcnt lgkmcnt(0)\n"
    "v_readfirstlane_b32 s52, v5\n"
    "s_add_u32 s52, s52, 380\n"
    "s_add_u32 s24, s52, -380\n"
    ".Ltw0:\n"
    "s_memrealtime s[50:51]\n"
    "s_waitcnt lgkmcnt(0)\n"
    "s_sub_u32 s50, s52, s50\n"
    "s_cmp_lt_u32 s50, 500\n"
    "s_cbranch_scc1 .Ltw0\n"
    ".Lw_37:\n"
    "ds_read_b32 v3, v2 offset:96\n"
    "s_waitcnt lgkmcnt(0)\n"
    "v_readfirstlane_b32 s48, v3\n"
    "s_cmp_ge_u32 s48, 4\n"
    "s_cbranch_scc1 .Ld_36\n"
    "s_add_u32 s40, s40, 1\n"
    "s_cmp_lt_u32 s40, 262144\n"
    "s_cbranch_scc1 .Lw_37\n"
    ".Ld_36:\n"
    "buffer_load_dwordx2 v[16:17], v9, s[4:7], s8 offen offset:0 sc1\n"
    "buffer_load_dwordx2 v[18:19], v9, s[4:7], s8 offen offset:1024 sc1\n"
    "buffer_load_dwordx2 v[20:21], v11, s[4:7], s8 offen offset:0 sc1\n"
    "buffer_load_dwordx2 v[22:23], v11, s[4:7], s8 offen offset:1024 sc1\n"
    "buffer_load_dwordx2 v[24:25], v9, s[4:7], s9 offen offset:0 sc1\n"
    "buffer_load_dwordx2 v[26:27], v9, s[4:7], s9 offen offset:1024 sc1\n"
    "buffer_load_dwordx2 v[28:29], v11, s[4:7], s9 offen offset:0 sc1\n"
    "buffer_load_dwordx2 v[30:31], v11, s[4:7], s9 offen offset:1024 sc1\n"
    "buffer_load_dwordx2 v[32:33], v9, s[4:7], s10 offen offset:0 sc1\n"
    "buffer_load_dwordx2 v[34:35], v9, s[4:7], s10 offen offset:1024 sc1\n"
    "buffer_load_dwordx2 v[36:37], v11, s[4:7], s10 offen offset:0 sc1\n"
    "buffer_load_dwordx2 v[38:39], v11, s[4:7], s10 offen offset:1024 sc1\n"
    "buffer_load_dwordx2 v[40:41], v9, s[4:7], s11 offen offset:0 sc1\n"
    "buffer_load_dwordx2 v[42:43], v9, s[4:7], s11 offen offset:1024 sc1\n"
    "buffer_load_dwordx2 v[44:45], v11, s[4:7], s11 offen offset:0 sc1\n"
    "buffer_load_dwordx2 v[46:47], v11, s[4:7], s11 offen offset:1024 sc1\n"
    ".Lh1:\n"
    ".Lw_39:\n"
    "ds_read_b32 v3, v2 offset:48\n"
    "s_waitcnt lgkmcnt(0)\n"
    "v_readfirstlane_b32 s48, v3\n"
    "s_cmp_ge_u32 s48, 8\n"
    "s_cbranch_scc1 .Ld_38\n"
    "s_add_u32 s40, s40, 1\n"
    "s_cmp_lt_u32 s40, 262144\n"
    "s_cbranch_scc1 .Lw_39\n"
    ".Ld_38:\n"
    "ds_read_b64_tr_b16 v[60:61], v7 offset:0\n"
    "ds_read_b64_tr_b16 v[62:63], v7 offset:3584\n"
    "ds_read_b64_tr_b16 v[64:65], v7 offset:7168\n"
    "ds_read_b64_tr_b16 v[66:67], v7 offset:10752\n"
    "ds_read_b64_tr_b16 v[68:69], v7 offset:14336\n"
    "ds_read_b64_tr_b16 v[70:71], v7 offset:17920\n"
    "ds_read_b64_tr_b16 v[72:73], v7 offset:21504\n"
    "ds_read_b64_tr_b16 v[74:75], v7 offset:25088\n"
    "ds_read_b64_tr_b16 v[76:77], v7 offset:28672\n"
    "ds_read_b64_tr_b16 v[78:79], v7 offset:32256\n"
    "ds_read_b64_tr_b16 v[80:81], v7 offset:35840\n"
    "ds_read_b64_tr_b16 v[82:83], v7 offset:39424\n"
    "ds_read_b64_tr_b16 v[84:85], v7 offset:43008\n"
    "ds_read_b64_tr_b16 v[86:87], v7 offset:46592\n"
    "s_waitcnt lgkmcnt(12)\n"
    "v_mfma_f32_16x16x32_bf16 v[52:55], v[60:63], v[48:51], 0\n"
    "ds_read_b64_tr_b16 v[88:89], v7 offset:50176\n"
    "ds_read_b64_tr_b16 v[90:91], v7 offset:53760\n"
    "s_mul_i32 s51, s55, 57344\n"
    "s_waitcnt lgkmcnt(12)\n"
    "v_mfma_f32_16x16x32_bf16 v[52:55], v[64:67], v[48:51], v[52:55]\n"
    "ds_read_b64_tr_b16 v[92:93], v8 offset:0\n"
    "ds_read_b64_tr_b16 v[94:95], v8 offset:3584\n"
    "v_add_u32 v6, s51, v6\n"
    "s_waitcnt lgkmcnt(12)\n"
    "v_mfma_f32_16x16x32_bf16 v[52:55], v[68:71], v[48:51], v[52:55]\n"
    "ds_read_b64_tr_b16 v[96:97], v8 offset:7168\n"
    "ds_read_b64_tr_b16 v[98:99], v8 offset:10752\n"
    "v_lshrrev_b32 v10, 1, v1\n"
    "s_waitcnt lgkmcnt(12)\n"
    "v_mfma_f32_16x16x32_bf16 v[52:55], v[72:75], v[48:51], v[52:55]\n"
    "ds_read_b64_tr_b16 v[100:101], v8 offset:14336\n"
    "ds_read_b64_tr_b16 v[102:103], v8 offset:17920\n"
    "v_bfe_u32 v15, v0, 4, 2\n"
    "s_waitcnt lgkmcnt(12)\n"
    "v_mfma_f32_16x16x32_bf16 v[52:55], v[76:79], v[48:51], v[52:55]\n"
    "ds_read_b64_tr_b16 v[104:105], v8 offset:21504\n"
    "ds_read_b64_tr_b16 v[106:107], v8 offset:25088\n"
    "s_add_u32 s58, s28, 2\n"
    "s_waitcnt lgkmcnt(12)\n"
    "v_mfma_f32_16x16x32_bf16 v[52:55], v[80:83], v[48:51], v[52:55]\n"
    "ds_read_b64_tr_b16 v[108:109], v8 offset:28672\n"
    "ds_read_b64_tr_b16 v[110:111], v8 offset:32256\n"
    "s_lshr_b32 s58, s58, 3\n"
    "s_waitcnt lgkmcnt(12)\n"
    "v_mfma_f32_16x16x32_bf16 v[52:55], v[84:87], v[48:51], v[52:55]\n"
    "ds_read_b64_tr_b16 v[112:113], v8 offset:35840\n"
    "ds_read_b64_tr_b16 v[114:115], v8 offset:39424\n"
    "s_mul_i32 s58, s58, -86016\n"
    "s_waitcnt lgkmcnt(12)\n"
    "v_mfma_f32_16x16x32_bf16 v[52:55], v[88:91], v[48:51], v[52:55]\n"
    "ds_read_b64_tr_b16 v[116:117], v8 offset:43008\n"
    "ds_read_b64_tr_b16 v[118:119], v8 offset:46592\n"
    "s_add_u32 s58, s58, 114688\n"
    "s_waitcnt lgkmcnt(12)\n"
    "v_mfma_f32_16x16x32_bf16 v[52:55], v[92:95], v[48:51], v[52:55]\n"
    "ds_read_b64_tr_b16 v[120:121], v8 offset:50176\n"
    "ds_read_b64_tr_b16 v[122:123], v8 offset:53760\n"
    "s_mul_i32 s51, s28, 7168\n"
    "s_waitcnt lgkmcnt(12)\n"
    "v_mfma_f32_16x16x32_bf16 v[52:55], v[96:99], v[48:51], v[52:55]\n"
    "s_add_u32 s51, s51, s58\n"
    "v_add_u32 v12, s51, v1\n"
    "s_lshl_b32 s49, s28, 6\n"
    "s_add_u32 s49, s49, 157696\n"
    "s_waitcnt lgkmcnt(10)\n"
    "v_mfma_f32_16x16x32_bf16 v[52:55], v[100:103], v[48:51], v[52:55]\n"
    "s_add_u32 s51, s54, 4\n"
    "s_and_b32 s51, s51, 15\n"
    "s_lshl_b32 s51, s51, 11\n"
    "s_add_u32 s12, s51, s53\n"
    "s_waitcnt lgkmcnt(8)\n"
    "v_mfma_f32_16x16x32_bf16 v[52:55], v[104:107], v[48:51], v[52:55]\n"
    "s_add_u32 s51, s54, 5\n"
    "s_and_b32 s51, s51, 15\n"
    "s_lshl_b32 s51, s51, 11\n"
    "s_add_u32 s13, s51, s53\n"
    "s_waitcnt lgkmcnt(6)\n"
    "v_mfma_f32_16x16x32_bf16 v[52:55], v[108:111], v[48:51], v[52:55]\n"
    "s_add_u32 s51, s54, 6\n"
    "s_and_b32 s51, s51, 15\n"
    "s_lshl_b32 s51, s51, 11\n"
    "s_add_u32 s14, s51, s53\n"
    "s_waitcnt lgkmcnt(4)\n"
    "v_mfma_f32_16x16x32_bf16 v[52:55], v[112:115], v[48:51], v[52:55]\n"
    "s_add_u32 s51, s54, 7\n"
    "s_and_b32 s51, s51, 15\n"
    "s_lshl_b32 s51, s51, 11\n"
    "s_add_u32 s15, s51, s53\n"
    "s_waitcnt lgkmcnt(2)\n"
    "v_mfma_f32_16x16x32_bf16 v[52:55], v[116:119], v[48:51], v[52:55]\n"
    "s_waitcnt lgkmcnt(0)\n"
    "v_mfma_f32_16x16x32_bf16 v[52:55], v[120:123], v[48:51], v[52:55]\n"
    "v_lshl_add_u32 v3, v15, 4, s49\n"
    "v_lshlrev_b32 v8, 4, v15\n"
    "v_add_u32 v8, 157696, v8\n"
    "s_lshl_b32 s51, s32, 7\n"
    "v_and_b32 v7, 0x78, v10\n"
    "v_add_u32 v7, s51, v7\n"
    "s_nop 1\n"
    "ds_write_b128 v3, v[52:55]\n"
    "s_mov_b64 exec, 1\n"
    "ds_inc_u32 v2, v2 offset:52\n"
    "s_mov_b64 exec, -1\n"
    "s_cmp_eq_u32 s36, 0\n"
    "s_cbranch_scc1 .Lh2\n"
    "s_waitcnt vmcnt(16)\n"
    "s_call_b64 s[56:57], .Lshuf0\n"
    "s_waitcnt vmcnt(12)\n"
    "s_call_b64 s[56:57], .Lshuf1\n"
    "s_waitcnt vmcnt(8)\n"
    "s_call_b64 s[56:57], .Lshuf2\n"
    "s_waitcnt vmcnt(4)\n"
    "s_call_b64 s[56:57], .Lshuf3\n"
    "s_waitcnt vmcnt(0)\n"
    "s_call_b64 s[56:57], .Lshuf4\n"
    "s_setprio 2\n"
    "s_waitcnt lgkmcnt(0)\n"
    "v_readfirstlane_b32 s52, v5\n"
    "s_add_u32 s52, s52, 520\n"
    "s_add_u32 s24, s52, -520\n"
    ".Ltw1:\n"
    "s_memrealtime s[50:51]\n"
    "s_waitcnt lgkmcnt(0)\n"
    "s_sub_u32 s50, s52, s50\n"
    "s_cmp_lt_u32 s50, 500\n"
    "s_cbranch_scc1 .Ltw1\n"
    "buffer_load_dwordx2 v[16:17], v9, s[4:7], s8 offen offset:0 sc1\n"
    "buffer_load_dwordx2 v[18:19], v9, s[4:7], s8 offen offset:1024 sc1\n"
    "buffer_load_dwordx2 v[20:21], v11, s[4:7], s8 offen offset:0 sc1\n"
    "buffer_load_dwordx2 v[22:23], v11, s[4:7], s8 offen offset:1024 sc1\n"
    "buffer_load_dwordx2 v[24:25], v9, s[4:7], s9 offen offset:0 sc1\n"
    "buffer_load_dwordx2 v[26:27], v9, s[4:7], s9 offen offset:1024 sc1\n"
    "buffer_load_dwordx2 v[28:29], v11, s[4:7], s9 offen offset:0 sc1\n"
    "buffer_load_dwordx2 v[30:31], v11, s[4:7], s9 offen offset:1024 sc1\n"
    "buffer_load_dwordx2 v[32:33], v9, s[4:7], s10 offen offset:0 sc1\n"
    "buffer_load_dwordx2 v[34:35], v9, s[4:7], s10 offen offset:1024 sc1\n"
    "buffer_load_dwordx2 v[36:37], v11, s[4:7], s10 offen offset:0 sc1\n"
    "buffer_load_dwordx2 v[38:39], v11, s[4:7], s10 offen offset:1024 sc1\n"
    "buffer_load_dwordx2 v[40:41], v9, s[4:7], s11 offen offset:0 sc1\n"
    "buffer_load_dwordx2 v[42:43], v9, s[4:7], s11 offen offset:1024 sc1\n"
    "buffer_load_dwordx2 v[44:45], v11, s[4:7], s11 offen offset:0 sc1\n"
    "buffer_load_dwordx2 v[46:47], v11, s[4:7], s11 offen offset:1024 sc1\n"
    "s_branch .Lh4\n"
    ".Lh2:\n"
    "buffer_load_dwordx2 v[224:225], v9, s[4:7], s12 offen offset:0 sc1\n"
    "buffer_load_dwordx2 v[226:227], v9, s[4:7], s12 offen offset:1024 sc1\n"
    "buffer_load_dwordx2 v[228:229], v11, s[4:7], s12 offen offset:0 sc1\n"
    "buffer_load_dwordx2 v[230:231], v11, s[4:7], s12 offen offset:1024 sc1\n"
    "buffer_load_dwordx2 v[232:233], v9, s[4:7], s13 offen offset:0 sc1\n"
    "buffer_load_dwordx2 v[234:235], v9, s[4:7], s13 offen offset:1024 sc1\n"
    "buffer_load_dwordx2 v[236:237], v11, s[4:7], s13 offen offset:0 sc1\n"
    "buffer_load_dwordx2 v[238:239], v11, s[4:7], s13 offen offset:1024 sc1\n"
    "buffer_load_dwordx2 v[240:241], v9, s[4:7], s14 offen offset:0 sc1\n"
    "buffer_load_dwordx2 v[242:243], v9, s[4:7], s14 offen offset:1024 sc1\n"
    "buffer_load_dwordx2 v[244:245], v11, s[4:7], s14 offen offset:0 sc1\n"
    "buffer_load_dwordx2 v[246:247], v11, s[4:7], s14 offen offset:1024 sc1\n"
    "buffer_load_dwordx2 v[248:249], v9, s[4:7], s15 offen offset:0 sc1\n"
    "buffer_load_dwordx2 v[250:251], v9, s[4:7], s15 offen offset:1024 sc1\n"
    "buffer_load_dwordx2 v[252:253], v11, s[4:7], s15 offen offset:0 sc1\n"
    "buffer_load_dwordx2 v[254:255], v11, s[4:7], s15 offen offset:1024 sc1\n"
    ".Lh4:\n"
    "ds_read_b64_tr_b16 v[116:117], v6 offset:0\n"
    "ds_read_b64_tr_b16 v[118:119], v6 offset:3584\n"
    "ds_read_b64_tr_b16 v[120:121], v6 offset:32\n"
    "ds_read_b64_tr_b16 v[122:123], v6 offset:3616\n"
    "ds_read_b64_tr_b16 v[124:125], v6 offset:64\n"
    "ds_read_b64_tr_b16 v[126:127], v6 offset:3648\n"
    "ds_read_b64_tr_b16 v[128:129], v6 offset:96\n"
    "ds_read_b64_tr_b16 v[130:131], v6 offset:3680\n"
    "ds_read_b64_tr_b16 v[132:133], v6 offset:128\n"
    "ds_read_b64_tr_b16 v[134:135], v6 offset:3712\n"
    "ds_read_b64_tr_b16 v[136:137], v6 offset:160\n"
    "ds_read_b64_tr_b16 v[138:139], v6 offset:3744\n"
    "ds_read_b64_tr_b16 v[140:141], v6 offset:192\n"
    "ds_read_b64_tr_b16 v[142:143], v6 offset:3776\n"
    "s_waitcnt vmcnt(28)\n"
    "s_cmp_eq_u32 s36, 0\n"
    "s_cbranch_scc1 .Lli_40\n"
    "s_waitcnt vmcnt(12)\n"
    ".Lli_40:\n"
    "v_max3_u32 v15, v16, v17, v18\n"
    "v_max3_u32 v15, v15, v19, v20\n"
    "v_max3_u32 v15, v15, v21, v22\n"
    "v_max_u32 v15, v15, v23\n"
    "v_cmp_eq_u32_e32 vcc, -1, v15\n"
    "s_cbranch_vccnz .Ls_41\n"
    ".Lb_42:\n"
    "s_mov_b64 exec, s[16:17]\n"
    "v_swap_b32 v17, v20\n"
    "v_swap_b32 v19, v22\n"
    "s_mov_b64 exec, s[18:19]\n"
    "v_swap_b32 v16, v21\n"
    "v_swap_b32 v18, v23\n"
    "s_mov_b64 exec, -1\n"
    "s_nop 0\n"
    "v_mfma_f32_16x16x32_bf16 v[56:59], v[16:19], v[16:19], 0\n"
    "v_mfma_f32_16x16x32_bf16 v[52:55], v[48:51], v[16:19], 0\n"
    "s_mov_b32 vcc_lo, 0xaaaaaaaa\n"
    "s_mov_b32 vcc_hi, 0xaaaaaaaa\n"
    "s_nop 4\n"
    "v_cndmask_b32_e32 v56, v56, v57, vcc\n"
    "v_cndmask_b32_e32 v58, v58, v59, vcc\n"
    "s_mov_b32 vcc_lo, 0xcccccccc\n"
    "s_mov_b32 vcc_hi, 0xcccccccc\n"
    "v_cndmask_b32_e32 v53, v56, v58, vcc\n"
    "v_min_u32 v53, -2, v53\n"
    "s_mov_b32 exec_lo, 0x00f0000f\n"
    "s_mov_b32 exec_hi, 0xf0000f00\n"
    "buffer_store_dwordx2 v[52:53], v7, s[4:7], s46 offen sc1\n"
    "s_mov_b64 exec, -1\n"
    "s_cmp_eq_u32 s36, 0\n"
    "s_cbranch_scc1 .Lli3\n"
    "buffer_load_dwordx2 v[224:225], v9, s[4:7], s12 offen offset:0 sc1\n"
    "buffer_load_dwordx2 v[226:227], v9, s[4:7], s12 offen offset:1024 sc1\n"
    "buffer_load_dwordx2 v[228:229], v11, s[4:7], s12 offen offset:0 sc1\n"
    "buffer_load_dwordx2 v[230:231], v11, s[4:7], s12 offen offset:1024 sc1\n"
    "buffer_load_dwordx2 v[232:233], v9, s[4:7], s13 offen offset:0 sc1\n"
    "buffer_load_dwordx2 v[234:235], v9, s[4:7], s13 offen offset:1024 sc1\n"
    "buffer_load_dwordx2 v[236:237], v11, s[4:7], s13 offen offset:0 sc1\n"
    "buffer_load_dwordx2 v[238:239], v11, s[4:7], s13 offen offset:1024 sc1\n"
    "buffer_load_dwordx2 v[240:241], v9, s[4:7], s14 offen offset:0 sc1\n"
    "buffer_load_dwordx2 v[242:243], v9, s[4:7], s14 offen offset:1024 sc1\n"
    "buffer_load_dwordx2 v[244:245], v11, s[4:7], s14 offen offset:0 sc1\n"
    "buffer_load_dwordx2 v[246:247], v11, s[4:7], s14 offen offset:1024 sc1\n"
    "buffer_load_dwordx2 v[248:249], v9, s[4:7], s15 offen offset:0 sc1\n"
    "buffer_load_dwordx2 v[250:251], v9, s[4:7], s15 offen offset:1024 sc1\n"
    "buffer_load_dwordx2 v[252:253], v11, s[4:7], s15 offen offset:0 sc1\n"
    "buffer_load_dwordx2 v[254:255], v11, s[4:7], s15 offen offset:1024 sc1\n"
    ".Lli3:\n"
    "s_waitcnt lgkmcnt(10)\n"
    "v_mfma_f32_16x16x32_bf16 v[60:63], v[116:119], v[16:19], 0\n"
    "v_mfma_f32_16x16x32_bf16 v[88:91], v[116:119], v[20:23], 0\n"
    "v_mfma_f32_16x16x32_bf16 v[64:67], v[120:123], v[16:19], 0\n"
    "ds_read_b64_tr_b16 v[116:117], v6 offset:7168\n"
    "v_mfma_f32_16x16x32_bf16 v[92:95], v[120:123], v[20:23], 0\n"
    "ds_read_b64_tr_b16 v[118:119], v6 offset:10752\n"
    "s_waitcnt lgkmcnt(8)\n"
    "v_mfma_f32_16x16x32_bf16 v[68:71], v[124:127], v[16:19], 0\n"
    "ds_read_b64_tr_b16 v[120:121], v6 offset:7200\n"
    "v_mfma_f32_16x16x32_bf16 v[96:99], v[124:127], v[20:23], 0\n"
    "ds_read_b64_tr_b16 v[122:123], v6 offset:10784\n"
    "s_waitcnt vmcnt(24)\n"
    "v_max3_u32 v15, v24, v25, v26\n"
    "v_mfma_f32_16x16x32_bf16 v[72:75], v[128:131], v[16:19], 0\n"
    "ds_read_b64_tr_b16 v[124:125], v6 offset:7232\n"
    "v_max3_u32 v15, v15, v27, v28\n"
    "v_mfma_f32_16x16x32_bf16 v[100:103], v[128:131], v[20:23], 0\n"
    "ds_read_b64_tr_b16 v[126:127], v6 offset:10816\n"
    "v_max3_u32 v15, v15, v29, v30\n"
    "s_waitcnt lgkmcnt(8)\n"
    "v_mfma_f32_16x16x32_bf16 v[76:79], v[132:135], v[16:19], 0\n"
    "ds_read_b64_tr_b16 v[128:129], v6 offset:7264\n"
    "v_max_u32 v15, v15, v31\n"
    "v_mfma_f32_16x16x32_bf16 v[104:107], v[132:135], v[20:23], 0\n"
    "ds_read_b64_tr_b16 v[130:131], v6 offset:10848\n"
    "v_cmp_eq_u32_e32 vcc, -1, v15\n"
    "v_mfma_f32_16x16x32_bf16 v[80:83], v[136:139], v[16:19], 0\n"
    "ds_read_b64_tr_b16 v[132:133], v6 offset:7296\n"
    "s_cbranch_vccnz .Ls_43\n"
    ".Lb_44:\n"
    "v_mfma_f32_16x16x32_bf16 v[108:111], v[136:139], v[20:23], 0\n"
    "ds_read_b64_tr_b16 v[134:135], v6 offset:10880\n"
    "s_mov_b64 exec, s[16:17]\n"
    "v_swap_b32 v25, v28\n"
    "v_swap_b32 v27, v30\n"
    "s_mov_b64 exec, s[18:19]\n"
    "v_swap_b32 v24, v29\n"
    "v_swap_b32 v26, v31\n"
    "s_mov_b64 exec, -1\n"
    "s_waitcnt lgkmcnt(10)\n"
    "v_mfma_f32_16x16x32_bf16 v[84:87], v[140:143], v[16:19], 0\n"
    "ds_read_b64_tr_b16 v[136:137], v6 offset:7328\n"
    "v_mfma_f32_16x16x32_bf16 v[112:115], v[140:143], v[20:23], 0\n"
    "ds_read_b64_tr_b16 v[138:139], v6 offset:10912\n"
    "s_waitcnt lgkmcnt(8)\n"
    "v_mfma_f32_16x16x32_bf16 v[60:63], v[116:119], v[24:27], v[60:63]\n"
    "ds_read_b64_tr_b16 v[140:141], v6 offset:7360\n"
    "v_mfma_f32_16x16x32_bf16 v[88:91], v[116:119], v[28:31], v[88:91]\n"
    "ds_read_b64_tr_b16 v[142:143], v6 offset:10944\n"
    "v_mfma_f32_16x16x32_bf16 v[64:67], v[120:123], v[24:27], v[64:67]\n"
    "ds_read_b64_tr_b16 v[116:117], v6 offset:14336\n"
    "v_mfma_f32_16x16x32_bf16 v[92:95], v[120:123], v[28:31], v[92:95]\n"
    "ds_read_b64_tr_b16 v[118:119], v6 offset:17920\n"
    "s_waitcnt lgkmcnt(8)\n"
    "v_mfma_f32_16x16x32_bf16 v[68:71], v[124:127], v[24:27], v[68:71]\n"
    "ds_read_b64_tr_b16 v[120:121], v6 offset:14368\n"
    "v_mfma_f32_16x16x32_bf16 v[96:99], v[124:127], v[28:31], v[96:99]\n"
    "ds_read_b64_tr_b16 v[122:123], v6 offset:17952\n"
    "s_waitcnt vmcnt(20)\n"
    "v_max3_u32 v15, v32, v33, v34\n"
    "v_mfma_f32_16x16x32_bf16 v[72:75], v[128:131], v[24:27], v[72:75]\n"
    "ds_read_b64_tr_b16 v[124:125], v6 offset:14400\n"
    "v_max3_u32 v15, v15, v35, v36\n"
    "v_mfma_f32_16x16x32_bf16 v[100:103], v[128:131], v[28:31], v[100:103]\n"
    "ds_read_b64_tr_b16 v[126:127], v6 offset:17984\n"
    "v_max3_u32 v15, v15, v37, v38\n"
    "s_waitcnt lgkmcnt(8)\n"
    "v_mfma_f32_16x16x32_bf16 v[76:79], v[132:135], v[24:27], v[76:79]\n"
    "ds_read_b64_tr_b16 v[128:129], v6 offset:14432\n"
    "v_max_u32 v15, v15, v39\n"
    "v_mfma_f32_16x16x32_bf16 v[104:107], v[132:135], v[28:31], v[104:107]\n"
    "ds_read_b64_tr_b16 v[130:131], v6 offset:18016\n"
    "v_cmp_eq_u32_e32 vcc, -1, v15\n"
    "v_mfma_f32_16x16x32_bf16 v[80:83], v[136:139], v[24:27], v[80:83]\n"
    "ds_read_b64_tr_b16 v[132:133], v6 offset:14464\n"
    "s_cbranch_vccnz .Ls_45\n"
    ".Lb_46:\n"
    "v_mfma_f32_16x16x32_bf16 v[108:111], v[136:139], v[28:31], v[108:111]\n"
    "ds_read_b64_tr_b16 v[134:135], v6 offset:18048\n"
    "s_mov_b64 exec, s[16:17]\n"
    "v_swap_b32 v33, v36\n"
    "v_swap_b32 v35, v38\n"
    "s_mov_b64 exec, s[18:19]\n"
    "v_swap_b32 v32, v37\n"
    "v_swap_b32 v34, v39\n"
    "s_mov_b64 exec, -1\n"
    "s_waitcnt lgkmcnt(10)\n"
    "v_mfma_f32_16x16x32_bf16 v[84:87], v[140:143], v[24:27], v[84:87]\n"
    "ds_read_b64_tr_b16 v[136:137], v6 offset:14496\n"
    "v_mfma_f32_16x16x32_bf16 v[112:115], v[140:143], v[28:31], v[112:115]\n"
    "ds_read_b64_tr_b16 v[138:139], v6 offset:18080\n"
    "s_waitcnt lgkmcnt(8)\n"
    "v_mfma_f32_16x16x32_bf16 v[60:63], v[116:119], v[32:35], v[60:63]\n"
    "ds_read_b64_tr_b16 v[140:141], v6 offset:14528\n"
    "v_mfma_f32_16x16x32_bf16 v[88:91], v[116:119], v[36:39], v[88:91]\n"
    "ds_read_b64_tr_b16 v[142:143], v6 offset:18112\n"
    "v_mfma_f32_16x16x32_bf16 v[64:67], v[120:123], v[32:35], v[64:67]\n"
    "ds_read_b64_tr_b16 v[116:117], v6 offset:21504\n"
    "v_mfma_f32_16x16x32_bf16 v[92:95], v[120:123], v[36:39], v[92:95]\n"
    "ds_read_b64_tr_b16 v[118:119], v6 offset:25088\n"
    "s_waitcnt lgkmcnt(8)\n"
    "v_mfma_f32_16x16x32_bf16 v[68:71], v[124:127], v[32:35], v[68:71]\n"
    "ds_read_b64_tr_b16 v[120:121], v6 offset:21536\n"
    "v_mfma_f32_16x16x32_bf16 v[96:99], v[124:127], v[36:39], v[96:99]\n"
    "ds_read_b64_tr_b16 v[122:123], v6 offset:25120\n"
    "s_waitcnt vmcnt(16)\n"
    "v_max3_u32 v15, v40, v41, v42\n"
    "v_mfma_f32_16x16x32_bf16 v[72:75], v[128:131], v[32:35], v[72:75]\n"
    "ds_read_b64_tr_b16 v[124:125], v6 offset:21568\n"
    "v_max3_u32 v15, v15, v43, v44\n"
    "v_mfma_f32_16x16x32_bf16 v[100:103], v[128:131], v[36:39], v[100:103]\n"
    "ds_read_b64_tr_b16 v[126:127], v6 offset:25152\n"
    "v_max3_u32 v15, v15, v45, v46\n"
    "s_waitcnt lgkmcnt(8)\n"
    "v_mfma_f32_16x16x32_bf16 v[76:79], v[132:135], v[32:35], v[76:79]\n"
    "ds_read_b64_tr_b16 v[128:129], v6 offset:21600\n"
    "v_max_u32 v15, v15, v47\n"
    "v_mfma_f32_16x16x32_bf16 v[104:107], v[132:135], v[36:39], v[104:107]\n"
    "ds_read_b64_tr_b16 v[130:131], v6 offset:25184\n"
    "v_cmp_eq_u32_e32 vcc, -1, v15\n"
    "v_mfma_f32_16x16x32_bf16 v[80:83], v[136:139], v[32:35], v[80:83]\n"
    "ds_read_b64_tr_b16 v[132:133], v6 offset:21632\n"
    "s_cbranch_vccnz .Ls_47\n"
    ".Lb_48:\n"
    "v_mfma_f32_16x16x32_bf16 v[108:111], v[136:139], v[36:39], v[108:111]\n"
    "ds_read_b64_tr_b16 v[134:135], v6 offset:25216\n"
    "s_mov_b64 exec, s[16:17]\n"
    "v_swap_b32 v41, v44\n"
    "v_swap_b32 v43, v46\n"
    "s_mov_b64 exec, s[18:19]\n"
    "v_swap_b32 v40, v45\n"
    "v_swap_b32 v42, v47\n"
    "s_mov_b64 exec, -1\n"
    "s_waitcnt lgkmcnt(10)\n"
    "v_mfma_f32_16x16x32_bf16 v[84:87], v[140:143], v[32:35], v[84:87]\n"
    "ds_read_b64_tr_b16 v[136:137], v6 offset:21664\n"
    "v_mfma_f32_16x16x32_bf16 v[112:115], v[140:143], v[36:39], v[112:115]\n"
    "ds_read_b64_tr_b16 v[138:139], v6 offset:25248\n"
    "s_waitcnt lgkmcnt(8)\n"
    "v_mfma_f32_16x16x32_bf16 v[60:63], v[116:119], v[40:43], v[60:63]\n"
    "ds_read_b64_tr_b16 v[140:141], v6 offset:21696\n"
    "v_mfma_f32_16x16x32_bf16 v[88:91], v[116:119], v[44:47], v[88:91]\n"
    "ds_read_b64_tr_b16 v[142:143], v6 offset:25280\n"
    "v_mfma_f32_16x16x32_bf16 v[64:67], v[120:123], v[40:43], v[64:67]\n"
    "ds_read_b64_tr_b16 v[116:117], v6 offset:28672\n"
    "v_mfma_f32_16x16x32_bf16 v[92:95], v[120:123], v[44:47], v[92:95]\n"
    "ds_read_b64_tr_b16 v[118:119], v6 offset:32256\n"
    "s_waitcnt lgkmcnt(8)\n"
    "v_mfma_f32_16x16x32_bf16 v[68:71], v[124:127], v[40:43], v[68:71]\n"
    "ds_read_b64_tr_b16 v[120:121], v6 offset:28704\n"
    "v_mfma_f32_16x16x32_bf16 v[96:99], v[124:127], v[44:47], v[96:99]\n"
    "ds_read_b64_tr_b16 v[122:123], v6 offset:32288\n"
    "s_waitcnt vmcnt(12)\n"
    "v_max3_u32 v15, v224, v225, v226\n"
    "v_mfma_f32_16x16x32_bf16 v[72:75], v[128:131], v[40:43], v[72:75]\n"
    "ds_read_b64_tr_b16 v[124:125], v6 offset:28736\n"
    "v_max3_u32 v15, v15, v227, v228\n"
    "v_mfma_f32_16x16x32_bf16 v[100:103], v[128:131], v[44:47], v[100:103]\n"
    "ds_read_b64_tr_b16 v[126:127], v6 offset:32320\n"
    "v_max3_u32 v15, v15, v229, v230\n"
    "s_waitcnt lgkmcnt(8)\n"
    "v_mfma_f32_16x16x32_bf16 v[76:79], v[132:135], v[40:43], v[76:79]\n"
    "ds_read_b64_tr_b16 v[128:129], v6 offset:28768\n"
    "v_max_u32 v15, v15, v231\n"
    "v_mfma_f32_16x16x32_bf16 v[104:107], v[132:135], v[44:47], v[104:107]\n"
    "ds_read_b64_tr_b16 v[130:131], v6 offset:32352\n"
    "v_cmp_eq_u32_e32 vcc, -1, v15\n"
    "v_mfma_f32_16x16x32_bf16 v[80:83], v[136:139], v[40:43], v[80:83]\n"
    "ds_read_b64_tr_b16 v[132:133], v6 offset:28800\n"
    "s_cbranch_vccnz .Ls_49\n"
    ".Lb_50:\n"
    "v_mfma_f32_16x16x32_bf16 v[108:111], v[136:139], v[44:47], v[108:111]\n"
    "ds_read_b64_tr_b16 v[134:135], v6 offset:32384\n"
    "s_mov_b64 exec, s[16:17]\n"
    "v_swap_b32 v225, v228\n"
    "v_swap_b32 v227, v230\n"
    "s_mov_b64 exec, s[18:19]\n"
    "v_swap_b32 v224, v229\n"
    "v_swap_b32 v226, v231\n"
    "s_mov_b64 exec, -1\n"
    "s_waitcnt lgkmcnt(10)\n"
    "v_mfma_f32_16x16x32_bf16 v[84:87], v[140:143], v[40:43], v[84:87]\n"
    "ds_read_b64_tr_b16 v[136:137], v6 offset:28832\n"
    "v_mfma_f32_16x16x32_bf16 v[112:115], v[140:143], v[44:47], v[112:115]\n"
    "ds_read_b64_tr_b16 v[138:139], v6 offset:32416\n"
    "s_waitcnt lgkmcnt(8)\n"
    "v_mfma_f32_16x16x32_bf16 v[60:63], v[116:119], v[224:227], v[60:63]\n"
    "ds_read_b64_tr_b16 v[140:141], v6 offset:28864\n"
    "s_bfe_u32 s54, s35, 0x10001\n"
    "v_mfma_f32_16x16x32_bf16 v[88:91], v[116:119], v[228:231], v[88:91]\n"
    "ds_read_b64_tr_b16 v[142:143], v6 offset:32448\n"
    "s_and_b32 s54, s54, 1\n"
    "v_mfma_f32_16x16x32_bf16 v[64:67], v[120:123], v[224:227], v[64:67]\n"
    "ds_read_b64_tr_b16 v[116:117], v6 offset:35840\n"
    "s_lshr_b32 s51, s34, 3\n"
    "v_mfma_f32_16x16x32_bf16 v[92:95], v[120:123], v[228:231], v[92:95]\n"
    "ds_read_b64_tr_b16 v[118:119], v6 offset:39424\n"
    "s_lshl_b32 s51, s51, 1\n"
    "s_waitcnt lgkmcnt(8)\n"
    "v_mfma_f32_16x16x32_bf16 v[68:71], v[124:127], v[224:227], v[68:71]\n"
    "ds_read_b64_tr_b16 v[120:121], v6 offset:35872\n"
    "s_lshr_b32 s52, s38, 2\n"
    "v_mfma_f32_16x16x32_bf16 v[96:99], v[124:127], v[228:231], v[96:99]\n"
    "ds_read_b64_tr_b16 v[122:123], v6 offset:39456\n"
    "s_waitcnt vmcnt(8)\n"
    "v_max3_u32 v15, v232, v233, v234\n"
    "s_add_u32 s51, s51, s52\n"
    "v_mfma_f32_16x16x32_bf16 v[72:75], v[128:131], v[224:227], v[72:75]\n"
    "ds_read_b64_tr_b16 v[124:125], v6 offset:35904\n"
    "v_max3_u32 v15, v15, v235, v236\n"
    "s_lshl_b32 s51, s51, 2\n"
    "v_mfma_f32_16x16x32_bf16 v[100:103], v[128:131], v[228:231], v[100:103]\n"
    "ds_read_b64_tr_b16 v[126:127], v6 offset:39488\n"
    "v_max3_u32 v15, v15, v237, v238\n"
    "s_bfe_u32 s52, s34, 0x20001\n"
    "s_waitcnt lgkmcnt(8)\n"
    "v_mfma_f32_16x16x32_bf16 v[76:79], v[132:135], v[224:227], v[76:79]\n"
    "ds_read_b64_tr_b16 v[128:129], v6 offset:35936\n"
    "v_max_u32 v15, v15, v239\n"
    "s_add_u32 s51, s51, s52\n"
    "v_mfma_f32_16x16x32_bf16 v[104:107], v[132:135], v[228:231], v[104:107]\n"
    "ds_read_b64_tr_b16 v[130:131], v6 offset:39520\n"
    "v_cmp_eq_u32_e32 vcc, -1, v15\n"
    "s_lshl_b32 s51, s51, 2\n"
    "v_mfma_f32_16x16x32_bf16 v[80:83], v[136:139], v[224:227], v[80:83]\n"
    "ds_read_b64_tr_b16 v[132:133], v6 offset:35968\n"
    "s_cbranch_vccnz .Ls_51\n"
    ".Lb_52:\n"
    "s_and_b32 s52, s38, 3\n"
    "v_mfma_f32_16x16x32_bf16 v[108:111], v[136:139], v[228:231], v[108:111]\n"
    "ds_read_b64_tr_b16 v[134:135], v6 offset:39552\n"
    "s_mov_b64 exec, s[16:17]\n"
    "v_swap_b32 v233, v236\n"
    "v_swap_b32 v235, v238\n"
    "s_mov_b64 exec, s[18:19]\n"
    "v_swap_b32 v232, v237\n"
    "v_swap_b32 v234, v239\n"
    "s_mov_b64 exec, -1\n"
    "s_add_u32 s51, s51, s52\n"
    "s_waitcnt lgkmcnt(10)\n"
    "v_mfma_f32_16x16x32_bf16 v[84:87], v[140:143], v[224:227], v[84:87]\n"
    "ds_read_b64_tr_b16 v[136:137], v6 offset:36000\n"
    "s_lshl_b32 s51, s51, 1\n"
    "v_mfma_f32_16x16x32_bf16 v[112:115], v[140:143], v[228:231], v[112:115]\n"
    "ds_read_b64_tr_b16 v[138:139], v6 offset:39584\n"
    "s_add_u32 s51, s51, s39\n"
    "s_waitcnt lgkmcnt(8)\n"
    "v_mfma_f32_16x16x32_bf16 v[60:63], v[116:119], v[232:235], v[60:63]\n"
    "ds_read_b64_tr_b16 v[140:141], v6 offset:36032\n"
    "s_mov_b64 exec, 1\n"
    "ds_inc_u32 v2, v2 offset:40\n"
    "s_mov_b64 exec, -1\n"
    "s_lshl_b32 s51, s51, 2\n"
    "v_mfma_f32_16x16x32_bf16 v[88:91], v[116:119], v[236:239], v[88:91]\n"
    "ds_read_b64_tr_b16 v[142:143], v6 offset:39616\n"
    "s_and_b32 s52, s34, 1\n"
    "v_mfma_f32_16x16x32_bf16 v[64:67], v[120:123], v[232:235], v[64:67]\n"
    "ds_read_b64_tr_b16 v[116:117], v6 offset:43008\n"
    "s_add_u32 s51, s51, s52\n"
    "v_mfma_f32_16x16x32_bf16 v[92:95], v[120:123], v[236:239], v[92:95]\n"
    "ds_read_b64_tr_b16 v[118:119], v6 offset:46592\n"
    "s_lshl_b32 s51, s51, 1\n"
    "s_waitcnt lgkmcnt(9)\n"
    "v_mfma_f32_16x16x32_bf16 v[68:71], v[124:127], v[232:235], v[68:71]\n"
    "ds_read_b64_tr_b16 v[120:121], v6 offset:43040\n"
    "s_add_u32 s51, s51, s54\n"
    "v_mfma_f32_16x16x32_bf16 v[96:99], v[124:127], v[236:239], v[96:99]\n"
    "ds_read_b64_tr_b16 v[122:123], v6 offset:46624\n"
    "s_waitcnt vmcnt(4)\n"
    "v_max3_u32 v15, v240, v241, v242\n"
    "s_mul_i32 s8, s51, 3584\n"
    "v_mfma_f32_16x16x32_bf16 v[72:75], v[128:131], v[232:235], v[72:75]\n"
    "ds_read_b64_tr_b16 v[124:125], v6 offset:43072\n"
    "v_max3_u32 v15, v15, v243, v244\n"
    "s_add_u32 s8, s8, 3145728\n"
    "v_mfma_f32_16x16x32_bf16 v[100:103], v[128:131], v[236:239], v[100:103]\n"
    "ds_read_b64_tr_b16 v[126:127], v6 offset:46656\n"
    "v_max3_u32 v15, v15, v245, v246\n"
    "s_xor_b32 s52, s51, 8\n"
    "s_waitcnt lgkmcnt(9)\n"
    "v_mfma_f32_16x16x32_bf16 v[76:79], v[132:135], v[232:235], v[76:79]\n"
    "ds_read_b64_tr_b16 v[128:129], v6 offset:43104\n"
    "v_max_u32 v15, v15, v247\n"
    "s_mul_i32 s9, s52, 3584\n"
    "v_mfma_f32_16x16x32_bf16 v[104:107], v[132:135], v[236:239], v[104:107]\n"
    "ds_read_b64_tr_b16 v[130:131], v6 offset:46688\n"
    "v_cmp_eq_u32_e32 vcc, -1, v15\n"
    "s_add_u32 s9, s9, 3145728\n"
    "v_mfma_f32_16x16x32_bf16 v[80:83], v[136:139], v[232:235], v[80:83]\n"
    "ds_read_b64_tr_b16 v[132:133], v6 offset:43136\n"
    "s_cbranch_vccnz .Ls_53\n"
    ".Lb_54:\n"
    "s_xor_b32 s52, s51, 1\n"
    "v_mfma_f32_16x16x32_bf16 v[108:111], v[136:139], v[236:239], v[108:111]\n"
    "ds_read_b64_tr_b16 v[134:135], v6 offset:46720\n"
    "s_mov_b64 exec, s[16:17]\n"
    "v_swap_b32 v241, v244\n"
    "v_swap_b32 v243, v246\n"
    "s_mov_b64 exec, s[18:19]\n"
    "v_swap_b32 v240, v245\n"
    "v_swap_b32 v242, v247\n"
    "s_mov_b64 exec, -1\n"
    "s_mul_i32 s10, s52, 3584\n"
    "s_waitcnt lgkmcnt(10)\n"
    "v_mfma_f32_16x16x32_bf16 v[84:87], v[140:143], v[232:235], v[84:87]\n"
    "ds_read_b64_tr_b16 v[136:137], v6 offset:43168\n"
    "s_add_u32 s10, s10, 3145728\n"
    "v_mfma_f32_16x16x32_bf16 v[112:115], v[140:143], v[236:239], v[112:115]\n"
    "ds_read_b64_tr_b16 v[138:139], v6 offset:46752\n"
    "s_xor_b32 s52, s51, 9\n"
    "s_waitcnt lgkmcnt(8)\n"
    "v_mfma_f32_16x16x32_bf16 v[60:63], v[116:119], v[240:243], v[60:63]\n"
    "ds_read_b64_tr_b16 v[140:141], v6 offset:43200\n"
    "buffer_load_dwordx2 v[52:53], v10, s[4:7], s46 offen offset:0 sc1\n"
    "buffer_load_dwordx2 v[54:55], v10, s[4:7], s46 offen offset:512 sc1\n"
    "buffer_load_dwordx2 v[56:57], v10, s[4:7], s46 offen offset:1024 sc1\n"
    "buffer_load_dwordx2 v[58:59], v10, s[4:7], s46 offen offset:1536 sc1\n"
    "s_mul_i32 s11, s52, 3584\n"
    "v_mfma_f32_16x16x32_bf16 v[88:91], v[116:119], v[244:247], v[88:91]\n"
    "ds_read_b64_tr_b16 v[142:143], v6 offset:46784\n"
    "s_add_u32 s11, s11, 3145728\n"
    "v_mfma_f32_16x16x32_bf16 v[64:67], v[120:123], v[240:243], v[64:67]\n"
    "ds_read_b64_tr_b16 v[116:117], v6 offset:50176\n"
    "s_lshl_b32 s51, s36, 5\n"
    "v_mfma_f32_16x16x32_bf16 v[92:95], v[120:123], v[244:247], v[92:95]\n"
    "ds_read_b64_tr_b16 v[118:119], v6 offset:53760\n"
    "s_add_u32 s51, s51, s34\n"
    "s_waitcnt lgkmcnt(8)\n"
    "v_mfma_f32_16x16x32_bf16 v[68:71], v[124:127], v[240:243], v[68:71]\n"
    "ds_read_b64_tr_b16 v[120:121], v6 offset:50208\n"
    "s_lshl_b32 s51, s51, 3\n"
    "v_mfma_f32_16x16x32_bf16 v[96:99], v[124:127], v[244:247], v[96:99]\n"
    "ds_read_b64_tr_b16 v[122:123], v6 offset:53792\n"
    "s_waitcnt vmcnt(4)\n"
    "v_max3_u32 v15, v248, v249, v250\n"
    "s_add_u32 s51, s51, s35\n"
    "v_mfma_f32_16x16x32_bf16 v[72:75], v[128:131], v[240:243], v[72:75]\n"
    "ds_read_b64_tr_b16 v[124:125], v6 offset:50240\n"
    "v_max3_u32 v15, v15, v251, v252\n"
    "ds_read_b32 v4, v14 offset:36\n"
    "ds_read_b32 v5, v2 offset:52\n"
    "s_lshl_b32 s51, s51, 2\n"
    "v_mfma_f32_16x16x32_bf16 v[100:103], v[128:131], v[244:247], v[100:103]\n"
    "ds_read_b64_tr_b16 v[126:127], v6 offset:53824\n"
    "v_max3_u32 v15, v15, v253, v254\n"
    "s_and_b32 s52, s28, 3\n"
    "s_waitcnt lgkmcnt(10)\n"
    "v_mfma_f32_16x16x32_bf16 v[76:79], v[132:135], v[240:243], v[76:79]\n"
    "ds_read_b64_tr_b16 v[128:129], v6 offset:50272\n"
    "v_max_u32 v15, v15, v255\n"
    "s_add_u32 s51, s51, s52\n"
    "v_mfma_f32_16x16x32_bf16 v[104:107], v[132:135], v[244:247], v[104:107]\n"
    "ds_read_b64_tr_b16 v[130:131], v6 offset:53856\n"
    "v_cmp_eq_u32_e32 vcc, -1, v15\n"
    "s_lshl_b32 s51, s51, 10\n"
    "v_mfma_f32_16x16x32_bf16 v[80:83], v[136:139], v[240:243], v[80:83]\n"
    "ds_read_b64_tr_b16 v[132:133], v6 offset:50304\n"
    "s_cbranch_vccnz .Ls_55\n"
    ".Lb_56:\n"
    "s_add_u32 s12, s51, 1048576\n"
    "v_mfma_f32_16x16x32_bf16 v[108:111], v[136:139], v[244:247], v[108:111]\n"
    "ds_read_b64_tr_b16 v[134:135], v6 offset:53888\n"
    "s_mov_b64 exec, s[16:17]\n"
    "v_swap_b32 v249, v252\n"
    "v_swap_b32 v251, v254\n"
    "s_mov_b64 exec, s[18:19]\n"
    "v_swap_b32 v248, v253\n"
    "v_swap_b32 v250, v255\n"
    "s_mov_b64 exec, -1\n"
    "s_waitcnt lgkmcnt(12)\n"
    "v_mfma_f32_16x16x32_bf16 v[84:87], v[140:143], v[240:243], v[84:87]\n"
    "ds_read_b64_tr_b16 v[136:137], v6 offset:50336\n"
    "v_mfma_f32_16x16x32_bf16 v[112:115], v[140:143], v[244:247], v[112:115]\n"
    "ds_read_b64_tr_b16 v[138:139], v6 offset:53920\n"
    "s_waitcnt lgkmcnt(10)\n"
    "v_mfma_f32_16x16x32_bf16 v[88:91], v[116:119], v[252:255], v[88:91]\n"
    "ds_read_b64_tr_b16 v[140:141], v6 offset:50368\n"
    "ds_read_b64_tr_b16 v[142:143], v6 offset:53952\n"
    "s_add_u32 s16, s8, 14336\n"
    "s_add_u32 s17, s9, 14336\n"
    "s_add_u32 s18, s10, 14336\n"
    "s_add_u32 s19, s11, 14336\n"
    "v_mfma_f32_16x16x32_bf16 v[92:95], v[120:123], v[252:255], v[92:95]\n"
    "s_waitcnt lgkmcnt(6)\n"
    "v_mfma_f32_16x16x32_bf16 v[96:99], v[124:127], v[252:255], v[96:99]\n"
    "v_lshl_add_u32 v11, s28, 2, v2\n"
    "v_xor_b32 v9, 4, v11\n"
    "v_mfma_f32_16x16x32_bf16 v[100:103], v[128:131], v[252:255], v[100:103]\n"
    "v_readfirstlane_b32 s48, v5\n"
    "s_cmp_ge_u32 s48, 8\n"
    "s_cbranch_scc0 .Ls_57\n"
    ".Lb_58:\n"
    "s_waitcnt lgkmcnt(2)\n"
    "v_mfma_f32_16x16x32_bf16 v[104:107], v[132:135], v[252:255], v[104:107]\n"
    "v_readfirstlane_b32 s48, v4\n"
    "s_cmp_ge_u32 s48, 8\n"
    "s_cbranch_scc0 .Ls_60\n"
    ".Lb_61:\n"
    "v_mfma_f32_16x16x32_bf16 v[108:111], v[136:139], v[252:255], v[108:111]\n"
    "ds_write_b128 v12, v[88:91] offset:0\n"
    "ds_read_b128 v[88:91], v8 offset:0\n"
    "s_waitcnt lgkmcnt(2)\n"
    "v_mfma_f32_16x16x32_bf16 v[112:115], v[140:143], v[252:255], v[112:115]\n"
    "ds_write_b128 v12, v[92:95] offset:1024\n"
    "ds_read_b128 v[92:95], v8 offset:64\n"
    "v_mfma_f32_16x16x32_bf16 v[60:63], v[116:119], v[248:251], v[60:63]\n"
    "ds_write_b128 v12, v[96:99] offset:2048\n"
    "ds_read_b128 v[96:99], v8 offset:128\n"
    "v_mfma_f32_16x16x32_bf16 v[64:67], v[120:123], v[248:251], v[64:67]\n"
    "ds_write_b128 v12, v[100:103] offset:3072\n"
    "ds_read_b128 v[100:103], v8 offset:192\n"
    "v_mfma_f32_16x16x32_bf16 v[68:71], v[124:127], v[248:251], v[68:71]\n"
    "ds_write_b128 v12, v[104:107] offset:4096\n"
    "ds_read_b128 v[104:107], v8 offset:256\n"
    "v_mfma_f32_16x16x32_bf16 v[72:75], v[128:131], v[248:251], v[72:75]\n"
    "ds_write_b128 v12, v[108:111] offset:5120\n"
    "ds_read_b128 v[108:111], v8 offset:320\n"
    "v_mfma_f32_16x16x32_bf16 v[76:79], v[132:135], v[248:251], v[76:79]\n"
    "ds_write_b128 v12, v[112:115] offset:6144\n"
    "ds_read_b128 v[112:115], v8 offset:384\n"
    "v_mfma_f32_16x16x32_bf16 v[80:83], v[136:139], v[248:251], v[80:83]\n"
    "s_mov_b64 exec, 1\n"
    "ds_inc_u32 v11, v2 offset:64\n"
    "s_mov_b64 exec, -1\n"
    "v_mfma_f32_16x16x32_bf16 v[84:87], v[140:143], v[248:251], v[84:87]\n"
    "s_waitcnt vmcnt(0)\n"
    "v_max3_u32 v15, v53, v55, v57\n"
    "v_max_u32 v15, v15, v59\n"
    "v_cmp_eq_u32_e32 vcc, -1, v15\n"
    "s_cbranch_vccnz .Ls_63\n"
    ".Lb_64:\n"
    "s_cmp_eq_u32 s36, 1\n"
    "s_cbranch_scc1 .Lep1\n"
    "s_setprio 1\n"
    "buffer_load_dwordx4 v[44:47], v1, s[4:7], s12 offen sc1\n"
    "s_add_u32 s45, s45, 32768\n"
    "buffer_load_dwordx4 v[176:179], v13, s[20:23], s45 offen offset:0\n"
    "buffer_load_dwordx4 v[180:183], v13, s[20:23], s45 offen offset:1024\n"
    "buffer_load_dwordx4 v[184:187], v13, s[20:23], s45 offen offset:2048\n"
    "buffer_load_dwordx4 v[188:191], v13, s[20:23], s45 offen offset:3072\n"
    "s_xor_b32 s45, s39, 1\n"
    "s_mul_i32 s45, s45, 114688\n"
    "buffer_load_dwordx4 v[192:195], v13, s[20:23], s45 offen offset:0\n"
    "buffer_load_dwordx4 v[196:199], v13, s[20:23], s45 offen offset:1024\n"
    "buffer_load_dwordx4 v[200:203], v13, s[20:23], s45 offen offset:2048\n"
    "buffer_load_dwordx4 v[204:207], v13, s[20:23], s45 offen offset:3072\n"
    "s_add_u32 s45, s45, 32768\n"
    "buffer_load_dwordx4 v[208:211], v13, s[20:23], s45 offen offset:0\n"
    "buffer_load_dwordx4 v[212:215], v13, s[20:23], s45 offen offset:1024\n"
    "buffer_load_dwordx4 v[216:219], v13, s[20:23], s45 offen offset:2048\n"
    "buffer_load_dwordx4 v[220:223], v13, s[20:23], s45 offen offset:3072\n"
    "s_add_u32 s45, s45, 32768\n"
    "buffer_load_dwordx4 v[224:227], v13, s[20:23], s45 offen offset:0\n"
    "buffer_load_dwordx4 v[228:231], v13, s[20:23], s45 offen offset:1024\n"
    "buffer_load_dwordx4 v[232:235], v13, s[20:23], s45 offen offset:2048\n"
    "buffer_load_dwordx4 v[236:239], v13, s[20:23], s45 offen offset:3072\n"
    "s_mul_i32 s45, s39, 114688\n"
    "s_mul_i32 s50, s39, -28672\n"
    "s_add_u32 s50, s50, 12288\n"
    "v_and_b32 v3, 8, v0\n"
    "v_mul_i32_i24 v3, s50, v3\n"
    "v_add3_u32 v13, v13, v3, s45\n"
    "s_mov_b32 s45, 98304\n"
    "buffer_load_dwordx4 v[240:243], v13, s[20:23], s45 offen offset:0\n"
    "buffer_load_dwordx4 v[244:247], v13, s[20:23], s45 offen offset:1024\n"
    "buffer_load_dwordx4 v[248:251], v13, s[20:23], s45 offen offset:2048\n"
    "buffer_load_dwordx4 v[252:255], v13, s[20:23], s45 offen offset:3072\n"
    "v_add_f32 v52, v52, v56\n"
    "v_add_f32 v53, v53, v57\n"
    "v_add_f32 v54, v54, v58\n"
    "v_add_f32 v55, v55, v59\n"
    "v_add_f32 v52, v52, v54\n"
    "v_add_f32 v53, v53, v55\n"
    "v_mov_b32 v54, v52\n"
    "v_mov_b32 v55, v53\n"
    "v_mov_b32 v50, 0xbfb8aa3b\n"
    "v_permlane32_swap_b32 v52, v54\n"
    "v_permlane32_swap_b32 v53, v55\n"
    "v_add_f32 v52, v52, v54\n"
    "v_add_f32 v53, v53, v55\n"
    "v_mov_b32 v54, v52\n"
    "v_mov_b32 v55, v53\n"
    "v_mov_b32 v50, 0xbfb8aa3b\n"
    "v_permlane16_swap_b32 v52, v54\n"
    "v_permlane16_swap_b32 v53, v55\n"
    "v_add_f32 v52, v52, v54\n"
    "v_add_f32 v53, v53, v55\n"
    "v_mul_f32 v52, 0x3b000000, v52\n"
    "v_mul_f32 v53, 0x3b000000, v53\n"
    "v_fma_f32 v53, -v52, v52, v53\n"
    "v_max_f32 v53, 0, v53\n"
    "v_cvt_pk_bf16_f32 v53, v53, v53\n"
    "v_lshlrev_b32 v53, 16, v53\n"
    "v_add_f32 v53, 0x3727c5ac, v53\n"
    "v_cvt_pk_bf16_f32 v53, v53, v53\n"
    "v_lshlrev_b32 v53, 16, v53\n"
    "v_sqrt_f32 v53, v53\n"
    "v_cvt_pk_bf16_f32 v52, v52, v52\n"
    "v_lshlrev_b32 v52, 16, v52\n"
    "v_cvt_pk_bf16_f32 v53, v53, v53\n"
    "v_lshlrev_b32 v53, 16, v53\n"
    "v_rcp_f32 v53, v53\n"
    "v_mul_f32 v52, 0x3fb8aa3b, v52\n"
    "v_mul_f32 v48, 0xbfb8aa3b, v53\n"
    "v_mul_f32 v49, v52, v53\n"
    "ds_read_b32 v4, v9 offset:64\n"
    "s_xor_b32 s51, s28, 1\n"
    "s_mul_i32 s51, s51, 7168\n"
    "s_add_u32 s51, s51, s58\n"
    "v_add_u32 v12, s51, v1\n"
    "s_waitcnt lgkmcnt(14)\n"
    "v_mul_f32 v88, v49, v88\n"
    "v_mul_f32 v89, v49, v89\n"
    "v_mul_f32 v90, v49, v90\n"
    "v_mul_f32 v91, v49, v91\n"
    "s_waitcnt lgkmcnt(12)\n"
    "v_mul_f32 v92, v49, v92\n"
    "v_mul_f32 v93, v49, v93\n"
    "v_mul_f32 v94, v49, v94\n"
    "v_mul_f32 v95, v49, v95\n"
    "s_waitcnt lgkmcnt(10)\n"
    "v_mul_f32 v96, v49, v96\n"
    "v_mul_f32 v97, v49, v97\n"
    "v_mul_f32 v98, v49, v98\n"
    "v_mul_f32 v99, v49, v99\n"
    "v_fmac_f32 v88, v48, v60\n"
    "v_fmac_f32 v89, v48, v61\n"
    "v_fmac_f32 v90, v48, v62\n"
    "v_fmac_f32 v91, v48, v63\n"
    "v_fmac_f32 v92, v48, v64\n"
    "v_fmac_f32 v93, v48, v65\n"
    "v_fmac_f32 v94, v48, v66\n"
    "v_fmac_f32 v95, v48, v67\n"
    "v_fmac_f32 v96, v48, v68\n"
    "v_fmac_f32 v97, v48, v69\n"
    "v_fmac_f32 v98, v48, v70\n"
    "v_fmac_f32 v99, v48, v71\n"
    "s_waitcnt lgkmcnt(0)\n"
    "v_readfirstlane_b32 s48, v4\n"
    "s_cmp_ge_u32 s48, 1\n"
    "s_cbranch_scc0 .Ls_65\n"
    ".Lb_66:\n"
    "ds_read_b128 v[116:119], v12 offset:0\n"
    "ds_read_b128 v[120:123], v12 offset:1024\n"
    "ds_read_b128 v[124:127], v12 offset:2048\n"
    "ds_read_b128 v[128:131], v12 offset:3072\n"
    "ds_read_b128 v[132:135], v12 offset:4096\n"
    "ds_read_b128 v[136:139], v12 offset:5120\n"
    "ds_read_b128 v[140:143], v12 offset:6144\n"
    "v_mul_f32 v100, v49, v100\n"
    "v_mul_f32 v101, v49, v101\n"
    "v_mul_f32 v102, v49, v102\n"
    "v_mul_f32 v103, v49, v103\n"
    "v_mul_f32 v104, v49, v104\n"
    "v_mul_f32 v105, v49, v105\n"
    "v_mul_f32 v106, v49, v106\n"
    "v_mul_f32 v107, v49, v107\n"
    "v_mul_f32 v108, v49, v108\n"
    "v_mul_f32 v109, v49, v109\n"
    "v_mul_f32 v110, v49, v110\n"
    "v_mul_f32 v111, v49, v111\n"
    "v_mul_f32 v112, v49, v112\n"
    "v_mul_f32 v113, v49, v113\n"
    "v_mul_f32 v114, v49, v114\n"
    "v_mul_f32 v115, v49, v115\n"
    "v_fmac_f32 v100, v48, v72\n"
    "v_fmac_f32 v101, v48, v73\n"
    "v_fmac_f32 v102, v48, v74\n"
    "v_fmac_f32 v103, v48, v75\n"
    "v_fmac_f32 v104, v48, v76\n"
    "v_fmac_f32 v105, v48, v77\n"
    "v_fmac_f32 v106, v48, v78\n"
    "v_fmac_f32 v107, v48, v79\n"
    "v_fmac_f32 v108, v48, v80\n"
    "v_fmac_f32 v109, v48, v81\n"
    "v_fmac_f32 v110, v48, v82\n"
    "v_fmac_f32 v111, v48, v83\n"
    "v_fmac_f32 v112, v48, v84\n"
    "v_fmac_f32 v113, v48, v85\n"
    "v_fmac_f32 v114, v48, v86\n"
    "v_fmac_f32 v115, v48, v87\n"
    "s_waitcnt lgkmcnt(6)\n"
    "v_fmac_f32 v88, v48, v116\n"
    "v_fmac_f32 v89, v48, v117\n"
    "v_fmac_f32 v90, v48, v118\n"
    "v_fmac_f32 v91, v48, v119\n"
    "s_waitcnt lgkmcnt(5)\n"
    "v_fmac_f32 v92, v48, v120\n"
    "v_fmac_f32 v93, v48, v121\n"
    "v_fmac_f32 v94, v48, v122\n"
    "v_fmac_f32 v95, v48, v123\n"
    "v_exp_f32 v116, v88\n"
    "v_exp_f32 v117, v89\n"
    "v_exp_f32 v118, v90\n"
    "v_exp_f32 v119, v91\n"
    "v_exp_f32 v120, v92\n"
    "v_exp_f32 v121, v93\n"
    "v_exp_f32 v122, v94\n"
    "v_exp_f32 v123, v95\n"
    "v_fma_f32 v116, v116, v50, v50\n"
    "v_fma_f32 v117, v117, v50, v50\n"
    "v_fma_f32 v118, v118, v50, v50\n"
    "v_fma_f32 v119, v119, v50, v50\n"
    "v_fma_f32 v120, v120, v50, v50\n"
    "v_fma_f32 v121, v121, v50, v50\n"
    "v_fma_f32 v122, v122, v50, v50\n"
    "v_fma_f32 v123, v123, v50, v50\n"
    "v_rcp_f32 v116, v116\n"
    "v_rcp_f32 v117, v117\n"
    "v_rcp_f32 v118, v118\n"
    "v_rcp_f32 v119, v119\n"
    "v_rcp_f32 v120, v120\n"
    "v_rcp_f32 v121, v121\n"
    "v_rcp_f32 v122, v122\n"
    "v_rcp_f32 v123, v123\n"
    "v_mul_f32 v116, v116, v88\n"
    "v_mul_f32 v117, v117, v89\n"
    "v_mul_f32 v118, v118, v90\n"
    "v_mul_f32 v119, v119, v91\n"
    "v_mul_f32 v120, v120, v92\n"
    "v_mul_f32 v121, v121, v93\n"
    "v_mul_f32 v122, v122, v94\n"
    "v_mul_f32 v123, v123, v95\n"
    "v_cvt_pk_bf16_f32 v116, v116, v117\n"
    "v_cvt_pk_bf16_f32 v117, v118, v119\n"
    "v_cvt_pk_bf16_f32 v118, v120, v121\n"
    "v_cvt_pk_bf16_f32 v119, v122, v123\n"
    "v_min_u32 v119, 0xbfffffff, v119\n"
    "buffer_store_dwordx4 v[116:119], v1, s[4:7], s44 offen offset:0 sc1\n"
    "s_waitcnt lgkmcnt(4)\n"
    "v_fmac_f32 v96, v48, v124\n"
    "v_fmac_f32 v97, v48, v125\n"
    "v_fmac_f32 v98, v48, v126\n"
    "v_fmac_f32 v99, v48, v127\n"
    "s_waitcnt lgkmcnt(3)\n"
    "v_fmac_f32 v100, v48, v128\n"
    "v_fmac_f32 v101, v48, v129\n"
    "v_fmac_f32 v102, v48, v130\n"
    "v_fmac_f32 v103, v48, v131\n"
    "v_exp_f32 v124, v96\n"
    "v_exp_f32 v125, v97\n"
    "v_exp_f32 v126, v98\n"
    "v_exp_f32 v127, v99\n"
    "v_exp_f32 v128, v100\n"
    "v_exp_f32 v129, v101\n"
    "v_exp_f32 v130, v102\n"
    "v_exp_f32 v131, v103\n"
    "v_fma_f32 v124, v124, v50, v50\n"
    "v_fma_f32 v125, v125, v50, v50\n"
    "v_fma_f32 v126, v126, v50, v50\n"
    "v_fma_f32 v127, v127, v50, v50\n"
    "v_fma_f32 v128, v128, v50, v50\n"
    "v_fma_f32 v129, v129, v50, v50\n"
    "v_fma_f32 v130, v130, v50, v50\n"
    "v_fma_f32 v131, v131, v50, v50\n"
    "v_rcp_f32 v124, v124\n"
    "v_rcp_f32 v125, v125\n"
    "v_rcp_f32 v126, v126\n"
    "v_rcp_f32 v127, v127\n"
    "v_rcp_f32 v128, v128\n"
    "v_rcp_f32 v129, v129\n"
    "v_rcp_f32 v130, v130\n"
    "v_rcp_f32 v131, v131\n"
    "v_mul_f32 v124, v124, v96\n"
    "v_mul_f32 v125, v125, v97\n"
    "v_mul_f32 v126, v126, v98\n"
    "v_mul_f32 v127, v127, v99\n"
    "v_mul_f32 v128, v128, v100\n"
    "v_mul_f32 v129, v129, v101\n"
    "v_mul_f32 v130, v130, v102\n"
    "v_mul_f32 v131, v131, v103\n"
    "v_cvt_pk_bf16_f32 v124, v124, v125\n"
    "v_cvt_pk_bf16_f32 v125, v126, v127\n"
    "v_cvt_pk_bf16_f32 v126, v128, v129\n"
    "v_cvt_pk_bf16_f32 v127, v130, v131\n"
    "v_min_u32 v127, 0xbfffffff, v127\n"
    "buffer_store_dwordx4 v[124:127], v1, s[4:7], s44 offen offset:1024 sc1\n"
    "s_waitcnt lgkmcnt(2)\n"
    "v_fmac_f32 v104, v48, v132\n"
    "v_fmac_f32 v105, v48, v133\n"
    "v_fmac_f32 v106, v48, v134\n"
    "v_fmac_f32 v107, v48, v135\n"
    "s_waitcnt lgkmcnt(1)\n"
    "v_fmac_f32 v108, v48, v136\n"
    "v_fmac_f32 v109, v48, v137\n"
    "v_fmac_f32 v110, v48, v138\n"
    "v_fmac_f32 v111, v48, v139\n"
    "v_exp_f32 v132, v104\n"
    "v_exp_f32 v133, v105\n"
    "v_exp_f32 v134, v106\n"
    "v_exp_f32 v135, v107\n"
    "v_exp_f32 v136, v108\n"
    "v_exp_f32 v137, v109\n"
    "v_exp_f32 v138, v110\n"
    "v_exp_f32 v139, v111\n"
    "v_fma_f32 v132, v132, v50, v50\n"
    "v_fma_f32 v133, v133, v50, v50\n"
    "v_fma_f32 v134, v134, v50, v50\n"
    "v_fma_f32 v135, v135, v50, v50\n"
    "v_fma_f32 v136, v136, v50, v50\n"
    "v_fma_f32 v137, v137, v50, v50\n"
    "v_fma_f32 v138, v138, v50, v50\n"
    "v_fma_f32 v139, v139, v50, v50\n"
    "v_rcp_f32 v132, v132\n"
    "v_rcp_f32 v133, v133\n"
    "v_rcp_f32 v134, v134\n"
    "v_rcp_f32 v135, v135\n"
    "v_rcp_f32 v136, v136\n"
    "v_rcp_f32 v137, v137\n"
    "v_rcp_f32 v138, v138\n"
    "v_rcp_f32 v139, v139\n"
    "v_mul_f32 v132, v132, v104\n"
    "v_mul_f32 v133, v133, v105\n"
    "v_mul_f32 v134, v134, v106\n"
    "v_mul_f32 v135, v135, v107\n"
    "v_mul_f32 v136, v136, v108\n"
    "v_mul_f32 v137, v137, v109\n"
    "v_mul_f32 v138, v138, v110\n"
    "v_mul_f32 v139, v139, v111\n"
    "v_cvt_pk_bf16_f32 v132, v132, v133\n"
    "v_cvt_pk_bf16_f32 v133, v134, v135\n"
    "v_cvt_pk_bf16_f32 v134, v136, v137\n"
    "v_cvt_pk_bf16_f32 v135, v138, v139\n"
    "v_min_u32 v135, 0xbfffffff, v135\n"
    "buffer_store_dwordx4 v[132:135], v1, s[4:7], s44 offen offset:2048 sc1\n"
    "s_waitcnt lgkmcnt(0)\n"
    "v_fmac_f32 v112, v48, v140\n"
    "v_fmac_f32 v113, v48, v141\n"
    "v_fmac_f32 v114, v48, v142\n"
    "v_fmac_f32 v115, v48, v143\n"
    "v_exp_f32 v140, v112\n"
    "v_exp_f32 v141, v113\n"
    "v_exp_f32 v142, v114\n"
    "v_exp_f32 v143, v115\n"
    "v_fma_f32 v140, v140, v50, v50\n"
    "v_fma_f32 v141, v141, v50, v50\n"
    "v_fma_f32 v142, v142, v50, v50\n"
    "v_fma_f32 v143, v143, v50, v50\n"
    "v_rcp_f32 v140, v140\n"
    "v_rcp_f32 v141, v141\n"
    "v_rcp_f32 v142, v142\n"
    "v_rcp_f32 v143, v143\n"
    "v_mul_f32 v140, v140, v112\n"
    "v_mul_f32 v141, v141, v113\n"
    "v_mul_f32 v142, v142, v114\n"
    "v_mul_f32 v143, v143, v115\n"
    "v_cvt_pk_bf16_f32 v140, v140, v141\n"
    "v_cvt_pk_bf16_f32 v141, v142, v143\n"
    "v_min_u32 v141, 0xbfffffff, v141\n"
    "buffer_store_dwordx2 v[140:141], v10, s[4:7], s44 offen offset:3072 sc1\n"
    "s_branch .Lep2\n"
    ".Lep1:\n"
    "s_setprio 1\n"
    "buffer_load_dwordx4 v[44:47], v1, s[4:7], s12 offen sc1\n"
    "s_add_u32 s45, s45, 32768\n"
    "buffer_load_dwordx4 v[224:227], v13, s[20:23], s45 offen offset:0\n"
    "buffer_load_dwordx4 v[228:231], v13, s[20:23], s45 offen offset:1024\n"
    "buffer_load_dwordx4 v[232:235], v13, s[20:23], s45 offen offset:2048\n"
    "buffer_load_dwordx4 v[236:239], v13, s[20:23], s45 offen offset:3072\n"
    "s_mul_i32 s45, s39, 114688\n"
    "s_mul_i32 s50, s39, -28672\n"
    "s_add_u32 s50, s50, 12288\n"
    "v_and_b32 v3, 8, v0\n"
    "v_mul_i32_i24 v3, s50, v3\n"
    "v_add3_u32 v13, v13, v3, s45\n"
    "s_mov_b32 s45, 98304\n"
    "buffer_load_dwordx4 v[240:243], v13, s[20:23], s45 offen offset:0\n"
    "buffer_load_dwordx4 v[244:247], v13, s[20:23], s45 offen offset:1024\n"
    "buffer_load_dwordx4 v[248:251], v13, s[20:23], s45 offen offset:2048\n"
    "buffer_load_dwordx4 v[252:255], v13, s[20:23], s45 offen offset:3072\n"
    "v_add_f32 v52, v52, v56\n"
    "v_add_f32 v53, v53, v57\n"
    "v_add_f32 v54, v54, v58\n"
    "v_add_f32 v55, v55, v59\n"
    "v_add_f32 v52, v52, v54\n"
    "v_add_f32 v53, v53, v55\n"
    "v_mov_b32 v54, v52\n"
    "v_mov_b32 v55, v53\n"
    "v_mov_b32 v50, 0xbfb8aa3b\n"
    "v_permlane32_swap_b32 v52, v54\n"
    "v_permlane32_swap_b32 v53, v55\n"
    "v_add_f32 v52, v52, v54\n"
    "v_add_f32 v53, v53, v55\n"
    "v_mov_b32 v54, v52\n"
    "v_mov_b32 v55, v53\n"
    "v_mov_b32 v50, 0xbfb8aa3b\n"
    "v_permlane16_swap_b32 v52, v54\n"
    "v_permlane16_swap_b32 v53, v55\n"
    "v_add_f32 v52, v52, v54\n"
    "v_add_f32 v53, v53, v55\n"
    "v_mul_f32 v52, 0x3b000000, v52\n"
    "v_mul_f32 v53, 0x3b000000, v53\n"
    "v_fma_f32 v53, -v52, v52, v53\n"
    "v_max_f32 v53, 0, v53\n"
    "v_cvt_pk_bf16_f32 v53, v53, v53\n"
    "v_lshlrev_b32 v53, 16, v53\n"
    "v_add_f32 v53, 0x3727c5ac, v53\n"
    "v_cvt_pk_bf16_f32 v53, v53, v53\n"
    "v_lshlrev_b32 v53, 16, v53\n"
    "v_sqrt_f32 v53, v53\n"
    "v_cvt_pk_bf16_f32 v52, v52, v52\n"
    "v_lshlrev_b32 v52, 16, v52\n"
    "v_cvt_pk_bf16_f32 v53, v53, v53\n"
    "v_lshlrev_b32 v53, 16, v53\n"
    "v_rcp_f32 v53, v53\n"
    "v_mul_f32 v52, 0x3fb8aa3b, v52\n"
    "v_mul_f32 v48, 0xbfb8aa3b, v53\n"
    "v_mul_f32 v49, v52, v53\n"
    "ds_read_b32 v4, v9 offset:64\n"
    "s_xor_b32 s51, s28, 1\n"
    "s_mul_i32 s51, s51, 7168\n"
    "s_add_u32 s51, s51, s58\n"
    "v_add_u32 v12, s51, v1\n"
    "s_waitcnt lgkmcnt(14)\n"
    "v_pk_mul_f32 v[88:89], v[48:49], v[88:89] op_sel:[1,0] op_sel_hi:[1,1]\n"
    "v_pk_mul_f32 v[90:91], v[48:49], v[90:91] op_sel:[1,0] op_sel_hi:[1,1]\n"
    "s_waitcnt lgkmcnt(12)\n"
    "v_pk_mul_f32 v[92:93], v[48:49], v[92:93] op_sel:[1,0] op_sel_hi:[1,1]\n"
    "v_pk_mul_f32 v[94:95], v[48:49], v[94:95] op_sel:[1,0] op_sel_hi:[1,1]\n"
    "s_waitcnt lgkmcnt(10)\n"
    "v_pk_mul_f32 v[96:97], v[48:49], v[96:97] op_sel:[1,0] op_sel_hi:[1,1]\n"
    "v_pk_mul_f32 v[98:99], v[48:49], v[98:99] op_sel:[1,0] op_sel_hi:[1,1]\n"
    "v_pk_fma_f32 v[88:89], v[48:49], v[60:61], v[88:89] op_sel_hi:[0,1,1]\n"
    "v_pk_fma_f32 v[90:91], v[48:49], v[62:63], v[90:91] op_sel_hi:[0,1,1]\n"
    "v_pk_fma_f32 v[92:93], v[48:49], v[64:65], v[92:93] op_sel_hi:[0,1,1]\n"
    "v_pk_fma_f32 v[94:95], v[48:49], v[66:67], v[94:95] op_sel_hi:[0,1,1]\n"
    "v_pk_fma_f32 v[96:97], v[48:49], v[68:69], v[96:97] op_sel_hi:[0,1,1]\n"
    "v_pk_fma_f32 v[98:99], v[48:49], v[70:71], v[98:99] op_sel_hi:[0,1,1]\n"
    "s_waitcnt lgkmcnt(0)\n"
    "v_readfirstlane_b32 s48, v4\n"
    "s_cmp_ge_u32 s48, 1\n"
    "s_cbranch_scc0 .Ls_68\n"
    ".Lb_69:\n"
    "ds_read_b128 v[116:119], v12 offset:0\n"
    "ds_read_b128 v[120:123], v12 offset:1024\n"
    "ds_read_b128 v[124:127], v12 offset:2048\n"
    "ds_read_b128 v[128:131], v12 offset:3072\n"
    "ds_read_b128 v[132:135], v12 offset:4096\n"
    "ds_read_b128 v[136:139], v12 offset:5120\n"
    "ds_read_b128 v[140:143], v12 offset:6144\n"
    "v_pk_mul_f32 v[100:101], v[48:49], v[100:101] op_sel:[1,0] op_sel_hi:[1,1]\n"
    "v_pk_mul_f32 v[102:103], v[48:49], v[102:103] op_sel:[1,0] op_sel_hi:[1,1]\n"
    "v_pk_mul_f32 v[104:105], v[48:49], v[104:105] op_sel:[1,0] op_sel_hi:[1,1]\n"
    "v_pk_mul_f32 v[106:107], v[48:49], v[106:107] op_sel:[1,0] op_sel_hi:[1,1]\n"
    "v_pk_mul_f32 v[108:109], v[48:49], v[108:109] op_sel:[1,0] op_sel_hi:[1,1]\n"
    "v_pk_mul_f32 v[110:111], v[48:49], v[110:111] op_sel:[1,0] op_sel_hi:[1,1]\n"
    "v_pk_mul_f32 v[112:113], v[48:49], v[112:113] op_sel:[1,0] op_sel_hi:[1,1]\n"
    "v_pk_mul_f32 v[114:115], v[48:49], v[114:115] op_sel:[1,0] op_sel_hi:[1,1]\n"
    "v_pk_fma_f32 v[100:101], v[48:49], v[72:73], v[100:101] op_sel_hi:[0,1,1]\n"
    "v_pk_fma_f32 v[102:103], v[48:49], v[74:75], v[102:103] op_sel_hi:[0,1,1]\n"
    "v_pk_fma_f32 v[104:105], v[48:49], v[76:77], v[104:105] op_sel_hi:[0,1,1]\n"
    "v_pk_fma_f32 v[106:107], v[48:49], v[78:79], v[106:107] op_sel_hi:[0,1,1]\n"
    "v_pk_fma_f32 v[108:109], v[48:49], v[80:81], v[108:109] op_sel_hi:[0,1,1]\n"
    "v_pk_fma_f32 v[110:111], v[48:49], v[82:83], v[110:111] op_sel_hi:[0,1,1]\n"
    "v_pk_fma_f32 v[112:113], v[48:49], v[84:85], v[112:113] op_sel_hi:[0,1,1]\n"
    "v_pk_fma_f32 v[114:115], v[48:49], v[86:87], v[114:115] op_sel_hi:[0,1,1]\n"
    "s_waitcnt lgkmcnt(6)\n"
    "v_pk_fma_f32 v[88:89], v[48:49], v[116:117], v[88:89] op_sel_hi:[0,1,1]\n"
    "v_pk_fma_f32 v[90:91], v[48:49], v[118:119], v[90:91] op_sel_hi:[0,1,1]\n"
    "s_waitcnt lgkmcnt(5)\n"
    "v_pk_fma_f32 v[92:93], v[48:49], v[120:121], v[92:93] op_sel_hi:[0,1,1]\n"
    "v_pk_fma_f32 v[94:95], v[48:49], v[122:123], v[94:95] op_sel_hi:[0,1,1]\n"
    "v_exp_f32 v116, v88\n"
    "v_exp_f32 v117, v89\n"
    "v_exp_f32 v118, v90\n"
    "v_exp_f32 v119, v91\n"
    "v_exp_f32 v120, v92\n"
    "v_exp_f32 v121, v93\n"
    "v_exp_f32 v122, v94\n"
    "v_exp_f32 v123, v95\n"
    "v_pk_fma_f32 v[116:117], v[116:117], v[50:51], v[50:51] op_sel_hi:[1,0,0]\n"
    "v_pk_fma_f32 v[118:119], v[118:119], v[50:51], v[50:51] op_sel_hi:[1,0,0]\n"
    "v_pk_fma_f32 v[120:121], v[120:121], v[50:51], v[50:51] op_sel_hi:[1,0,0]\n"
    "v_pk_fma_f32 v[122:123], v[122:123], v[50:51], v[50:51] op_sel_hi:[1,0,0]\n"
    "v_rcp_f32 v116, v116\n"
    "v_rcp_f32 v117, v117\n"
    "v_rcp_f32 v118, v118\n"
    "v_rcp_f32 v119, v119\n"
    "v_rcp_f32 v120, v120\n"
    "v_rcp_f32 v121, v121\n"
    "v_rcp_f32 v122, v122\n"
    "v_rcp_f32 v123, v123\n"
    "v_pk_mul_f32 v[116:117], v[116:117], v[88:89]\n"
    "v_pk_mul_f32 v[118:119], v[118:119], v[90:91]\n"
    "v_pk_mul_f32 v[120:121], v[120:121], v[92:93]\n"
    "v_pk_mul_f32 v[122:123], v[122:123], v[94:95]\n"
    "v_cvt_pk_bf16_f32 v116, v116, v117\n"
    "v_cvt_pk_bf16_f32 v117, v118, v119\n"
    "v_cvt_pk_bf16_f32 v118, v120, v121\n"
    "v_cvt_pk_bf16_f32 v119, v122, v123\n"
    "v_min_u32 v119, 0xbfffffff, v119\n"
    "buffer_store_dwordx4 v[116:119], v1, s[4:7], s44 offen offset:0 sc1\n"
    "s_waitcnt lgkmcnt(4)\n"
    "v_pk_fma_f32 v[96:97], v[48:49], v[124:125], v[96:97] op_sel_hi:[0,1,1]\n"
    "v_pk_fma_f32 v[98:99], v[48:49], v[126:127], v[98:99] op_sel_hi:[0,1,1]\n"
    "s_waitcnt lgkmcnt(3)\n"
    "v_pk_fma_f32 v[100:101], v[48:49], v[128:129], v[100:101] op_sel_hi:[0,1,1]\n"
    "v_pk_fma_f32 v[102:103], v[48:49], v[130:131], v[102:103] op_sel_hi:[0,1,1]\n"
    "v_exp_f32 v124, v96\n"
    "v_exp_f32 v125, v97\n"
    "v_exp_f32 v126, v98\n"
    "v_exp_f32 v127, v99\n"
    "v_exp_f32 v128, v100\n"
    "v_exp_f32 v129, v101\n"
    "v_exp_f32 v130, v102\n"
    "v_exp_f32 v131, v103\n"
    "v_pk_fma_f32 v[124:125], v[124:125], v[50:51], v[50:51] op_sel_hi:[1,0,0]\n"
    "v_pk_fma_f32 v[126:127], v[126:127], v[50:51], v[50:51] op_sel_hi:[1,0,0]\n"
    "v_pk_fma_f32 v[128:129], v[128:129], v[50:51], v[50:51] op_sel_hi:[1,0,0]\n"
    "v_pk_fma_f32 v[130:131], v[130:131], v[50:51], v[50:51] op_sel_hi:[1,0,0]\n"
    "v_rcp_f32 v124, v124\n"
    "v_rcp_f32 v125, v125\n"
    "v_rcp_f32 v126, v126\n"
    "v_rcp_f32 v127, v127\n"
    "v_rcp_f32 v128, v128\n"
    "v_rcp_f32 v129, v129\n"
    "v_rcp_f32 v130, v130\n"
    "v_rcp_f32 v131, v131\n"
    "v_pk_mul_f32 v[124:125], v[124:125], v[96:97]\n"
    "v_pk_mul_f32 v[126:127], v[126:127], v[98:99]\n"
    "v_pk_mul_f32 v[128:129], v[128:129], v[100:101]\n"
    "v_pk_mul_f32 v[130:131], v[130:131], v[102:103]\n"
    "v_cvt_pk_bf16_f32 v124, v124, v125\n"
    "v_cvt_pk_bf16_f32 v125, v126, v127\n"
    "v_cvt_pk_bf16_f32 v126, v128, v129\n"
    "v_cvt_pk_bf16_f32 v127, v130, v131\n"
    "v_min_u32 v127, 0xbfffffff, v127\n"
    "buffer_store_dwordx4 v[124:127], v1, s[4:7], s44 offen offset:1024 sc1\n"
    "s_waitcnt lgkmcnt(2)\n"
    "v_pk_fma_f32 v[104:105], v[48:49], v[132:133], v[104:105] op_sel_hi:[0,1,1]\n"
    "v_pk_fma_f32 v[106:107], v[48:49], v[134:135], v[106:107] op_sel_hi:[0,1,1]\n"
    "s_waitcnt lgkmcnt(1)\n"
    "v_pk_fma_f32 v[108:109], v[48:49], v[136:137], v[108:109] op_sel_hi:[0,1,1]\n"
    "v_pk_fma_f32 v[110:111], v[48:49], v[138:139], v[110:111] op_sel_hi:[0,1,1]\n"
    "v_exp_f32 v132, v104\n"
    "v_exp_f32 v133, v105\n"
    "v_exp_f32 v134, v106\n"
    "v_exp_f32 v135, v107\n"
    "v_exp_f32 v136, v108\n"
    "v_exp_f32 v137, v109\n"
    "v_exp_f32 v138, v110\n"
    "v_exp_f32 v139, v111\n"
    "v_pk_fma_f32 v[132:133], v[132:133], v[50:51], v[50:51] op_sel_hi:[1,0,0]\n"
    "v_pk_fma_f32 v[134:135], v[134:135], v[50:51], v[50:51] op_sel_hi:[1,0,0]\n"
    "v_pk_fma_f32 v[136:137], v[136:137], v[50:51], v[50:51] op_sel_hi:[1,0,0]\n"
    "v_pk_fma_f32 v[138:139], v[138:139], v[50:51], v[50:51] op_sel_hi:[1,0,0]\n"
    "v_rcp_f32 v132, v132\n"
    "v_rcp_f32 v133, v133\n"
    "v_rcp_f32 v134, v134\n"
    "v_rcp_f32 v135, v135\n"
    "v_rcp_f32 v136, v136\n"
    "v_rcp_f32 v137, v137\n"
    "v_rcp_f32 v138, v138\n"
    "v_rcp_f32 v139, v139\n"
    "v_pk_mul_f32 v[132:133], v[132:133], v[104:105]\n"
    "v_pk_mul_f32 v[134:135], v[134:135], v[106:107]\n"
    "v_pk_mul_f32 v[136:137], v[136:137], v[108:109]\n"
    "v_pk_mul_f32 v[138:139], v[138:139], v[110:111]\n"
    "v_cvt_pk_bf16_f32 v132, v132, v133\n"
    "v_cvt_pk_bf16_f32 v133, v134, v135\n"
    "v_cvt_pk_bf16_f32 v134, v136, v137\n"
    "v_cvt_pk_bf16_f32 v135, v138, v139\n"
    "v_min_u32 v135, 0xbfffffff, v135\n"
    "buffer_store_dwordx4 v[132:135], v1, s[4:7], s44 offen offset:2048 sc1\n"
    "s_waitcnt lgkmcnt(0)\n"
    "v_pk_fma_f32 v[112:113], v[48:49], v[140:141], v[112:113] op_sel_hi:[0,1,1]\n"
    "v_pk_fma_f32 v[114:115], v[48:49], v[142:143], v[114:115] op_sel_hi:[0,1,1]\n"
    "v_exp_f32 v140, v112\n"
    "v_exp_f32 v141, v113\n"
    "v_exp_f32 v142, v114\n"
    "v_exp_f32 v143, v115\n"
    "v_pk_fma_f32 v[140:141], v[140:141], v[50:51], v[50:51] op_sel_hi:[1,0,0]\n"
    "v_pk_fma_f32 v[142:143], v[142:143], v[50:51], v[50:51] op_sel_hi:[1,0,0]\n"
    "v_rcp_f32 v140, v140\n"
    "v_rcp_f32 v141, v141\n"
    "v_rcp_f32 v142, v142\n"
    "v_rcp_f32 v143, v143\n"
    "v_pk_mul_f32 v[140:141], v[140:141], v[112:113]\n"
    "v_pk_mul_f32 v[142:143], v[142:143], v[114:115]\n"
    "v_cvt_pk_bf16_f32 v140, v140, v141\n"
    "v_cvt_pk_bf16_f32 v141, v142, v143\n"
    "v_min_u32 v141, 0xbfffffff, v141\n"
    "buffer_store_dwordx2 v[140:141], v10, s[4:7], s44 offen offset:3072 sc1\n"
    ".Lep2:\n"
    "s_memrealtime s[48:49]\n"
    "s_waitcnt lgkmcnt(0)\n"
    "v_mov_b32 v3, s48\n"
    "s_mov_b64 exec, 1\n"
    "ds_max_u32 v2, v3 offset:12\n"
    "s_mov_b64 exec, 1\n"
    "ds_inc_u32 v2, v2 offset:8\n"
    "s_mov_b64 exec, -1\n"
    "s_setprio 0\n"
    "v_and_b32 v11, 15, v0\n"
    "v_bfe_u32 v12, v0, 4, 2\n"
    "s_mul_i32 s51, s28, 8704\n"
    "v_mul_u32_u24 v6, 272, v11\n"
    "v_lshl_add_u32 v6, v12, 4, v6\n"
    "v_add_u32 v6, s51, v6\n"
    "s_mul_i32 s51, s54, 4352\n"
    "s_sub_u32 s52, 4352, s51\n"
    "v_add_u32 v14, s52, v6\n"
    "v_add_u32 v6, s51, v6\n"
    "v_and_b32 v3, 4, v0\n"
    "v_and_b32 v11, 8, v0\n"
    "v_lshl_add_u32 v7, v3, 2, v11\n"
    "v_add_u32 v7, s28, v7\n"
    "v_mul_u32_u24 v7, 272, v7\n"
    "v_lshl_add_u32 v7, v12, 6, v7\n"
    "v_lshlrev_b32 v9, 10, v3\n"
    "v_lshl_add_u32 v9, v11, 11, v9\n"
    "v_lshl_add_u32 v8, v11, 3, v3\n"
    "v_lshl_add_u32 v9, v12, 4, v9\n"
    "v_and_b32 v3, 3, v0\n"
    "v_lshl_add_u32 v7, v3, 4, v7\n"
    "v_and_b32 v3, 1, v0\n"
    "v_lshl_add_u32 v8, v3, 11, v8\n"
    "v_lshl_add_u32 v9, v3, 6, v9\n"
    "v_and_b32 v3, 2, v0\n"
    "v_lshl_add_u32 v8, v3, 6, v8\n"
    "v_lshl_add_u32 v9, v3, 2, v9\n"
    "v_and_b32 v3, 16, v0\n"
    "v_lshl_add_u32 v8, v3, 4, v8\n"
    "v_and_b32 v3, 32, v0\n"
    "v_lshl_add_u32 v8, v3, 5, v8\n"
    "s_and_b32 s51, s28, 1\n"
    "s_lshl_b32 s51, s51, 3\n"
    "s_lshr_b32 s52, s28, 1\n"
    "s_add_u32 s51, s51, s52\n"
    "s_lshl_b32 s51, s51, 10\n"
    "s_lshl_b32 s52, s34, 15\n"
    "s_add_u32 s51, s51, s52\n"
    "s_lshl_b32 s53, s35, 7\n"
    "s_add_u32 s51, s51, s53\n"
    "v_add_u32 v9, s51, v9\n"
    "s_lshl_b32 s53, s28, 3\n"
    "s_add_u32 s52, s53, 139264\n"
    "v_add_u32 v8, s52, v8\n"
    "s_lshl_b32 s53, s28, 10\n"
    "s_add_u32 s52, s53, 139264\n"
    "v_add_u32 v11, s52, v1\n"
    "s_cmp_eq_u32 s36, 1\n"
    "s_cbranch_scc1 .LD0L\n"
    "s_setprio 0\n"
    "s_waitcnt vmcnt(20)\n"
    "s_call_b64 s[56:57], .Lshuf2\n"
    "s_waitcnt vmcnt(16)\n"
    "s_call_b64 s[56:57], .Lshuf3\n"
    "s_waitcnt vmcnt(12)\n"
    "s_call_b64 s[56:57], .Lshuf4\n"
    "s_waitcnt vmcnt(8)\n"
    "s_call_b64 s[56:57], .Lshuf5\n"
    "s_waitcnt vmcnt(4)\n"
    "s_call_b64 s[56:57], .Lshuf6\n"
    "s_cmp_eq_u32 s29, 0\n"
    "s_cbranch_scc0 .Lprobe\n"
    "v_mov_b32 v3, s37\n"
    "s_mov_b64 exec, 1\n"
    "buffer_store_dword v3, off, s[4:7], 0 sc1\n"
    "s_mov_b64 exec, -1\n"
    ".Lprobe:\n"
    "s_memrealtime s[50:51]\n"
    "s_waitcnt lgkmcnt(0)\n"
    "s_sub_u32 s50, s50, s48\n"
    "s_cmp_lt_u32 s50, 60\n"
    "s_cbranch_scc1 .Lprobe\n"
    "s_branch .LD0M\n"
    ".LD0L:\n"
    ".LD0M:\n"
    ".Lw_72:\n"
    "ds_read_b32 v3, v2 offset:8\n"
    "s_waitcnt lgkmcnt(0)\n"
    "v_readfirstlane_b32 s48, v3\n"
    "s_cmp_ge_u32 s48, 8\n"
    "s_cbranch_scc1 .Ld_71\n"
    "s_add_u32 s40, s40, 1\n"
    "s_cmp_lt_u32 s40, 262144\n"
    "s_cbranch_scc1 .Lw_72\n"
    ".Ld_71:\n"
    ".LD0:\n"
    "ds_read_b32 v59, v2 offset:12\n"
    "buffer_load_dwordx4 v[60:63], v1, s[4:7], s8 offen offset:0 sc1\n"
    "buffer_load_dwordx4 v[64:67], v1, s[4:7], s8 offen offset:1024 sc1\n"
    "buffer_load_dwordx4 v[68:71], v1, s[4:7], s8 offen offset:2048 sc1\n"
    "buffer_load_dwordx2 v[84:85], v10, s[4:7], s8 offen offset:3072 sc1\n"
    "buffer_load_dwordx4 v[72:75], v1, s[4:7], s9 offen offset:0 sc1\n"
    "buffer_load_dwordx4 v[76:79], v1, s[4:7], s9 offen offset:1024 sc1\n"
    "buffer_load_dwordx4 v[80:83], v1, s[4:7], s9 offen offset:2048 sc1\n"
    "buffer_load_dwordx2 v[86:87], v10, s[4:7], s9 offen offset:3072 sc1\n"
    "buffer_load_dwordx4 v[16:19], v1, s[4:7], s10 offen offset:0 sc1\n"
    "buffer_load_dwordx4 v[20:23], v1, s[4:7], s10 offen offset:1024 sc1\n"
    "buffer_load_dwordx4 v[24:27], v1, s[4:7], s10 offen offset:2048 sc1\n"
    "buffer_load_dwordx2 v[40:41], v10, s[4:7], s10 offen offset:3072 sc1\n"
    "buffer_load_dwordx4 v[28:31], v1, s[4:7], s11 offen offset:0 sc1\n"
    "buffer_load_dwordx4 v[32:35], v1, s[4:7], s11 offen offset:1024 sc1\n"
    "buffer_load_dwordx4 v[36:39], v1, s[4:7], s11 offen offset:2048 sc1\n"
    "buffer_load_dwordx2 v[42:43], v10, s[4:7], s11 offen offset:3072 sc1\n"
    "s_cmp_eq_u32 s36, 0\n"
    "s_cbranch_scc1 .LD1\n"
    "s_waitcnt vmcnt(24)\n"
    "s_call_b64 s[56:57], .Lshuf5\n"
    "s_waitcnt vmcnt(20)\n"
    "s_call_b64 s[56:57], .Lshuf6\n"
    "s_setprio 0\n"
    ".LD1:\n"
    "s_waitcnt vmcnt(12)\n"
    "v_max3_u32 v15, v63, v67, v71\n"
    "v_max_u32 v15, v15, v85\n"
    "v_cmp_le_u32_e32 vcc, 0xc0000000, v15\n"
    "s_cbranch_vccnz .Ls_73\n"
    ".Lb_74:\n"
    "s_setprio 3\n"
    "v_mfma_f32_16x16x32_bf16 v[88:91], v[144:147], v[60:63], 0\n"
    "v_mfma_f32_16x16x32_bf16 v[92:95], v[148:151], v[60:63], 0\n"
    "v_max3_u32 v15, v44, v45, v46\n"
    "v_max_u32 v15, v15, v47\n"
    "v_cmp_eq_u32_e32 vcc, -1, v15\n"
    "s_cbranch_vccnz .Ls_75\n"
    ".Lb_76:\n"
    "ds_write_b128 v11, v[44:47]\n"
    "v_mfma_f32_16x16x32_bf16 v[96:99], v[152:155], v[60:63], 0\n"
    "v_mfma_f32_16x16x32_bf16 v[100:103], v[156:159], v[60:63], 0\n"
    "v_mfma_f32_16x16x32_bf16 v[88:91], v[160:163], v[64:67], v[88:91]\n"
    "v_mfma_f32_16x16x32_bf16 v[92:95], v[164:167], v[64:67], v[92:95]\n"
    "v_mfma_f32_16x16x32_bf16 v[96:99], v[168:171], v[64:67], v[96:99]\n"
    "v_mfma_f32_16x16x32_bf16 v[100:103], v[172:175], v[64:67], v[100:103]\n"
    "v_mfma_f32_16x16x32_bf16 v[88:91], v[176:179], v[68:71], v[88:91]\n"
    "s_waitcnt vmcnt(8)\n"
    "v_max3_u32 v15, v75, v79, v83\n"
    "v_max_u32 v15, v15, v87\n"
    "v_mfma_f32_16x16x32_bf16 v[92:95], v[180:183], v[68:71], v[92:95]\n"
    "v_cmp_le_u32_e32 vcc, 0xc0000000, v15\n"
    "v_mfma_f32_16x16x32_bf16 v[96:99], v[184:187], v[68:71], v[96:99]\n"
    "s_cbranch_vccnz .Ls_77\n"
    ".Lb_78:\n"
    "v_mfma_f32_16x16x32_bf16 v[100:103], v[188:191], v[68:71], v[100:103]\n"
    "v_mfma_f32_16x16x32_bf16 v[88:91], v[192:195], v[72:75], v[88:91]\n"
    "v_mfma_f32_16x16x32_bf16 v[92:95], v[196:199], v[72:75], v[92:95]\n"
    "v_mfma_f32_16x16x32_bf16 v[96:99], v[200:203], v[72:75], v[96:99]\n"
    "v_mfma_f32_16x16x32_bf16 v[100:103], v[204:207], v[72:75], v[100:103]\n"
    "v_mfma_f32_16x16x32_bf16 v[88:91], v[208:211], v[76:79], v[88:91]\n"
    "s_memrealtime s[50:51]\n"
    "v_mfma_f32_16x16x32_bf16 v[92:95], v[212:215], v[76:79], v[92:95]\n"
    "v_mfma_f32_16x16x32_bf16 v[96:99], v[216:219], v[76:79], v[96:99]\n"
    "v_mfma_f32_16x16x32_bf16 v[100:103], v[220:223], v[76:79], v[100:103]\n"
    "s_waitcnt lgkmcnt(0)\n"
    "v_readfirstlane_b32 s52, v59\n"
    "s_sub_u32 s50, s50, s52\n"
    "s_cmp_lt_u32 s50, 110\n"
    "s_cbranch_scc1 .Ltw2\n"
    ".Ltw3:\n"
    "buffer_load_dwordx4 v[120:123], v1, s[4:7], s16 offen offset:0 sc1\n"
    "v_mfma_f32_16x16x32_bf16 v[88:91], v[224:227], v[80:83], v[88:91]\n"
    "v_mfma_f32_16x16x32_bf16 v[92:95], v[228:231], v[80:83], v[92:95]\n"
    "v_mfma_f32_16x16x32_bf16 v[96:99], v[232:235], v[80:83], v[96:99]\n"
    "buffer_load_dwordx4 v[124:127], v1, s[4:7], s16 offen offset:1024 sc1\n"
    "v_mfma_f32_16x16x32_bf16 v[100:103], v[236:239], v[80:83], v[100:103]\n"
    "v_mfma_f32_16x16x32_bf16 v[88:91], v[240:243], v[84:87], v[88:91]\n"
    "s_waitcnt vmcnt(6)\n"
    "v_max3_u32 v15, v19, v23, v27\n"
    "v_max_u32 v15, v15, v41\n"
    "v_mfma_f32_16x16x32_bf16 v[92:95], v[244:247], v[84:87], v[92:95]\n"
    "v_cmp_le_u32_e32 vcc, 0xc0000000, v15\n"
    "buffer_load_dwordx4 v[128:131], v1, s[4:7], s16 offen offset:2048 sc1\n"
    "v_mfma_f32_16x16x32_bf16 v[96:99], v[248:251], v[84:87], v[96:99]\n"
    "s_cbranch_vccnz .Ls_79\n"
    ".Lb_80:\n"
    "v_mfma_f32_16x16x32_bf16 v[100:103], v[252:255], v[84:87], v[100:103]\n"
    "v_mfma_f32_16x16x32_bf16 v[104:107], v[144:147], v[16:19], 0\n"
    "buffer_load_dwordx2 v[44:45], v10, s[4:7], s16 offen offset:3072 sc1\n"
    "v_mfma_f32_16x16x32_bf16 v[108:111], v[148:151], v[16:19], 0\n"
    "v_mfma_f32_16x16x32_bf16 v[112:115], v[152:155], v[16:19], 0\n"
    "ds_write_b128 v6, v[88:91] offset:0\n"
    "v_mfma_f32_16x16x32_bf16 v[116:119], v[156:159], v[16:19], 0\n"
    "buffer_load_dwordx4 v[132:135], v1, s[4:7], s17 offen offset:0 sc1\n"
    "ds_write_b128 v6, v[92:95] offset:64\n"
    "v_mfma_f32_16x16x32_bf16 v[104:107], v[160:163], v[20:23], v[104:107]\n"
    "ds_write_b128 v6, v[96:99] offset:128\n"
    "v_mfma_f32_16x16x32_bf16 v[108:111], v[164:167], v[20:23], v[108:111]\n"
    "ds_write_b128 v6, v[100:103] offset:192\n"
    "v_mfma_f32_16x16x32_bf16 v[112:115], v[168:171], v[20:23], v[112:115]\n"
    "buffer_load_dwordx4 v[136:139], v1, s[4:7], s17 offen offset:1024 sc1\n"
    "v_mfma_f32_16x16x32_bf16 v[116:119], v[172:175], v[20:23], v[116:119]\n"
    "v_mfma_f32_16x16x32_bf16 v[104:107], v[176:179], v[24:27], v[104:107]\n"
    "s_waitcnt vmcnt(6)\n"
    "v_max3_u32 v15, v31, v35, v39\n"
    "v_max_u32 v15, v15, v43\n"
    "v_mfma_f32_16x16x32_bf16 v[108:111], v[180:183], v[24:27], v[108:111]\n"
    "v_cmp_le_u32_e32 vcc, 0xc0000000, v15\n"
    "buffer_load_dwordx4 v[140:143], v1, s[4:7], s17 offen offset:2048 sc1\n"
    "v_mfma_f32_16x16x32_bf16 v[112:115], v[184:187], v[24:27], v[112:115]\n"
    "s_cbranch_vccnz .Ls_81\n"
    ".Lb_82:\n"
    "v_mfma_f32_16x16x32_bf16 v[116:119], v[188:191], v[24:27], v[116:119]\n"
    "s_setprio 2\n"
    "v_mfma_f32_16x16x32_bf16 v[104:107], v[192:195], v[28:31], v[104:107]\n"
    "buffer_load_dwordx2 v[46:47], v10, s[4:7], s17 offen offset:3072 sc1\n"
    "v_mfma_f32_16x16x32_bf16 v[108:111], v[196:199], v[28:31], v[108:111]\n"
    "v_mfma_f32_16x16x32_bf16 v[112:115], v[200:203], v[28:31], v[112:115]\n"
    "v_mfma_f32_16x16x32_bf16 v[116:119], v[204:207], v[28:31], v[116:119]\n"
    "buffer_load_dwordx4 v[60:63], v1, s[4:7], s18 offen offset:0 sc1\n"
    "v_mfma_f32_16x16x32_bf16 v[104:107], v[208:211], v[32:35], v[104:107]\n"
    "v_mfma_f32_16x16x32_bf16 v[108:111], v[212:215], v[32:35], v[108:111]\n"
    "v_mfma_f32_16x16x32_bf16 v[112:115], v[216:219], v[32:35], v[112:115]\n"
    "buffer_load_dwordx4 v[64:67], v1, s[4:7], s18 offen offset:1024 sc1\n"
    "v_mfma_f32_16x16x32_bf16 v[116:119], v[220:223], v[32:35], v[116:119]\n"
    "v_mfma_f32_16x16x32_bf16 v[104:107], v[224:227], v[36:39], v[104:107]\n"
    "v_mfma_f32_16x16x32_bf16 v[108:111], v[228:231], v[36:39], v[108:111]\n"
    "buffer_load_dwordx4 v[68:71], v1, s[4:7], s18 offen offset:2048 sc1\n"
    "v_mfma_f32_16x16x32_bf16 v[112:115], v[232:235], v[36:39], v[112:115]\n"
    "v_mfma_f32_16x16x32_bf16 v[116:119], v[236:239], v[36:39], v[116:119]\n"
    "v_mfma_f32_16x16x32_bf16 v[104:107], v[240:243], v[40:43], v[104:107]\n"
    "s_waitcnt vmcnt(7)\n"
    "v_max3_u32 v15, v123, v127, v131\n"
    "v_max_u32 v15, v15, v45\n"
    "buffer_load_dwordx2 v[84:85], v10, s[4:7], s18 offen offset:3072 sc1\n"
    "v_mfma_f32_16x16x32_bf16 v[108:111], v[244:247], v[40:43], v[108:111]\n"
    "v_cmp_le_u32_e32 vcc, 0xc0000000, v15\n"
    "v_mfma_f32_16x16x32_bf16 v[112:115], v[248:251], v[40:43], v[112:115]\n"
    "s_cbranch_vccnz .Ls_83\n"
    ".Lb_84:\n"
    "v_mfma_f32_16x16x32_bf16 v[116:119], v[252:255], v[40:43], v[116:119]\n"
    "buffer_load_dwordx4 v[72:75], v1, s[4:7], s19 offen offset:0 sc1\n"
    "v_mfma_f32_16x16x32_bf16 v[88:91], v[144:147], v[120:123], 0\n"
    "v_add_u32 v6, 69632, v6\n"
    "v_mfma_f32_16x16x32_bf16 v[92:95], v[148:151], v[120:123], 0\n"
    "v_mfma_f32_16x16x32_bf16 v[96:99], v[152:155], v[120:123], 0\n"
    "buffer_load_dwordx4 v[76:79], v1, s[4:7], s19 offen offset:1024 sc1\n"
    "ds_write_b128 v14, v[104:107] offset:0\n"
    "v_mfma_f32_16x16x32_bf16 v[100:103], v[156:159], v[120:123], 0\n"
    "ds_write_b128 v14, v[108:111] offset:64\n"
    "v_mfma_f32_16x16x32_bf16 v[88:91], v[160:163], v[124:127], v[88:91]\n"
    "ds_write_b128 v14, v[112:115] offset:128\n"
    "v_mfma_f32_16x16x32_bf16 v[92:95], v[164:167], v[124:127], v[92:95]\n"
    "buffer_load_dwordx4 v[80:83], v1, s[4:7], s19 offen offset:2048 sc1\n"
    "ds_write_b128 v14, v[116:119] offset:192\n"
    "v_mfma_f32_16x16x32_bf16 v[96:99], v[168:171], v[124:127], v[96:99]\n"
    "s_mov_b64 exec, 1\n"
    "ds_inc_u32 v2, v2 offset:60\n"
    "s_mov_b64 exec, -1\n"
    "v_add_u32 v14, 69632, v14\n"
    "v_mfma_f32_16x16x32_bf16 v[100:103], v[172:175], v[124:127], v[100:103]\n"
    "v_mfma_f32_16x16x32_bf16 v[88:91], v[176:179], v[128:131], v[88:91]\n"
    "s_waitcnt vmcnt(7)\n"
    "v_max3_u32 v15, v135, v139, v143\n"
    "v_max_u32 v15, v15, v47\n"
    "buffer_load_dwordx2 v[86:87], v10, s[4:7], s19 offen offset:3072 sc1\n"
    "v_mfma_f32_16x16x32_bf16 v[92:95], v[180:183], v[128:131], v[92:95]\n"
    "v_cmp_le_u32_e32 vcc, 0xc0000000, v15\n"
    "v_mfma_f32_16x16x32_bf16 v[96:99], v[184:187], v[128:131], v[96:99]\n"
    "s_cbranch_vccnz .Ls_85\n"
    ".Lb_86:\n"
    "v_mfma_f32_16x16x32_bf16 v[100:103], v[188:191], v[128:131], v[100:103]\n"
    "s_setprio 1\n"
    "v_mfma_f32_16x16x32_bf16 v[88:91], v[192:195], v[132:135], v[88:91]\n"
    "v_mfma_f32_16x16x32_bf16 v[92:95], v[196:199], v[132:135], v[92:95]\n"
    "v_mfma_f32_16x16x32_bf16 v[96:99], v[200:203], v[132:135], v[96:99]\n"
    "v_mfma_f32_16x16x32_bf16 v[100:103], v[204:207], v[132:135], v[100:103]\n"
    "v_mfma_f32_16x16x32_bf16 v[88:91], v[208:211], v[136:139], v[88:91]\n"
    "v_mfma_f32_16x16x32_bf16 v[92:95], v[212:215], v[136:139], v[92:95]\n"
    "v_mfma_f32_16x16x32_bf16 v[96:99], v[216:219], v[136:139], v[96:99]\n"
    "v_mfma_f32_16x16x32_bf16 v[100:103], v[220:223], v[136:139], v[100:103]\n"
    "v_mfma_f32_16x16x32_bf16 v[88:91], v[224:227], v[140:143], v[88:91]\n"
    "v_mfma_f32_16x16x32_bf16 v[92:95], v[228:231], v[140:143], v[92:95]\n"
    "v_mfma_f32_16x16x32_bf16 v[96:99], v[232:235], v[140:143], v[96:99]\n"
    "v_mfma_f32_16x16x32_bf16 v[100:103], v[236:239], v[140:143], v[100:103]\n"
    "v_mfma_f32_16x16x32_bf16 v[88:91], v[240:243], v[44:47], v[88:91]\n"
    "s_waitcnt vmcnt(4)\n"
    "v_max3_u32 v15, v63, v67, v71\n"
    "v_max_u32 v15, v15, v85\n"
    "v_mfma_f32_16x16x32_bf16 v[92:95], v[244:247], v[44:47], v[92:95]\n"
    "v_cmp_le_u32_e32 vcc, 0xc0000000, v15\n"
    "v_mfma_f32_16x16x32_bf16 v[96:99], v[248:251], v[44:47], v[96:99]\n"
    "s_cbranch_vccnz .Ls_87\n"
    ".Lb_88:\n"
    "v_mfma_f32_16x16x32_bf16 v[100:103], v[252:255], v[44:47], v[100:103]\n"
    "v_mfma_f32_16x16x32_bf16 v[104:107], v[144:147], v[60:63], 0\n"
    "ds_read_b32 v4, v2 offset:60\n"
    "v_mfma_f32_16x16x32_bf16 v[108:111], v[148:151], v[60:63], 0\n"
    "v_mfma_f32_16x16x32_bf16 v[112:115], v[152:155], v[60:63], 0\n"
    "ds_write_b128 v6, v[88:91] offset:0\n"
    "v_mfma_f32_16x16x32_bf16 v[116:119], v[156:159], v[60:63], 0\n"
    "ds_write_b128 v6, v[92:95] offset:64\n"
    "s_waitcnt lgkmcnt(2)\n"
    "v_readfirstlane_b32 s48, v4\n"
    "s_cmp_ge_u32 s48, 8\n"
    "s_cbranch_scc0 .Ls_89\n"
    ".Lb_90:\n"
    "v_mfma_f32_16x16x32_bf16 v[104:107], v[160:163], v[64:67], v[104:107]\n"
    "ds_write_b128 v6, v[96:99] offset:128\n"
    "ds_read2st64_b32 v[48:49], v8 offset0:0 offset1:2\n"
    "ds_read2st64_b32 v[50:51], v8 offset0:16 offset1:18\n"
    "ds_read_b128 v[16:19], v7 offset:0\n"
    "v_mfma_f32_16x16x32_bf16 v[108:111], v[164:167], v[64:67], v[108:111]\n"
    "ds_write_b128 v6, v[100:103] offset:192\n"
    "ds_read_b128 v[20:23], v7 offset:8704\n"
    "ds_read_b128 v[24:27], v7 offset:17408\n"
    "ds_read_b128 v[28:31], v7 offset:26112\n"
    "v_mfma_f32_16x16x32_bf16 v[112:115], v[168:171], v[64:67], v[112:115]\n"
    "ds_read_b128 v[32:35], v7 offset:34816\n"
    "ds_read_b128 v[36:39], v7 offset:43520\n"
    "ds_read_b128 v[40:43], v7 offset:52224\n"
    "v_mfma_f32_16x16x32_bf16 v[116:119], v[172:175], v[64:67], v[116:119]\n"
    "ds_read_b128 v[120:123], v7 offset:60928\n"
    "s_waitcnt lgkmcnt(9)\n"
    "v_lshlrev_b32 v52, 16, v48\n"
    "v_mfma_f32_16x16x32_bf16 v[104:107], v[176:179], v[68:71], v[104:107]\n"
    "s_waitcnt vmcnt(0)\n"
    "v_max3_u32 v15, v75, v79, v83\n"
    "v_max_u32 v15, v15, v87\n"
    "v_mfma_f32_16x16x32_bf16 v[108:111], v[180:183], v[68:71], v[108:111]\n"
    "v_cmp_le_u32_e32 vcc, 0xc0000000, v15\n"
    "v_mfma_f32_16x16x32_bf16 v[112:115], v[184:187], v[68:71], v[112:115]\n"
    "s_cbranch_vccnz .Ls_92\n"
    ".Lb_93:\n"
    "v_mfma_f32_16x16x32_bf16 v[116:119], v[188:191], v[68:71], v[116:119]\n"
    "v_and_b32 v53, s60, v48\n"
    "v_lshlrev_b32 v54, 16, v49\n"
    "v_and_b32 v55, s60, v49\n"
    "s_setprio 0\n"
    "v_mfma_f32_16x16x32_bf16 v[104:107], v[192:195], v[72:75], v[104:107]\n"
    "s_waitcnt lgkmcnt(6)\n"
    "v_add_f32 v16, v16, v20\n"
    "v_add_f32 v17, v17, v21\n"
    "v_mfma_f32_16x16x32_bf16 v[108:111], v[196:199], v[72:75], v[108:111]\n"
    "v_add_f32 v18, v18, v22\n"
    "v_add_f32 v19, v19, v23\n"
    "s_waitcnt lgkmcnt(4)\n"
    "v_mfma_f32_16x16x32_bf16 v[112:115], v[200:203], v[72:75], v[112:115]\n"
    "v_add_f32 v24, v24, v28\n"
    "v_add_f32 v25, v25, v29\n"
    "v_add_f32 v26, v26, v30\n"
    "v_mfma_f32_16x16x32_bf16 v[116:119], v[204:207], v[72:75], v[116:119]\n"
    "v_add_f32 v27, v27, v31\n"
    "v_add_f32 v16, v16, v24\n"
    "v_add_f32 v17, v17, v25\n"
    "v_mfma_f32_16x16x32_bf16 v[104:107], v[208:211], v[76:79], v[104:107]\n"
    "v_add_f32 v18, v18, v26\n"
    "v_add_f32 v19, v19, v27\n"
    "s_waitcnt lgkmcnt(2)\n"
    "v_mfma_f32_16x16x32_bf16 v[108:111], v[212:215], v[76:79], v[108:111]\n"
    "v_add_f32 v32, v32, v36\n"
    "v_add_f32 v33, v33, v37\n"
    "v_add_f32 v34, v34, v38\n"
    "v_mfma_f32_16x16x32_bf16 v[112:115], v[216:219], v[76:79], v[112:115]\n"
    "v_add_f32 v35, v35, v39\n"
    "s_waitcnt lgkmcnt(0)\n"
    "v_add_f32 v40, v40, v120\n"
    "v_mfma_f32_16x16x32_bf16 v[116:119], v[220:223], v[76:79], v[116:119]\n"
    "v_add_f32 v41, v41, v121\n"
    "v_add_f32 v42, v42, v122\n"
    "v_add_f32 v43, v43, v123\n"
    "v_mfma_f32_16x16x32_bf16 v[104:107], v[224:227], v[80:83], v[104:107]\n"
    "v_add_f32 v32, v32, v40\n"
    "v_add_f32 v33, v33, v41\n"
    "v_add_f32 v34, v34, v42\n"
    "v_mfma_f32_16x16x32_bf16 v[108:111], v[228:231], v[80:83], v[108:111]\n"
    "v_add_f32 v35, v35, v43\n"
    "v_add_f32 v16, v16, v32\n"
    "v_add_f32 v17, v17, v33\n"
    "v_mfma_f32_16x16x32_bf16 v[112:115], v[232:235], v[80:83], v[112:115]\n"
    "v_add_f32 v18, v18, v34\n"
    "v_add_f32 v19, v19, v35\n"
    "s_nop 0\n"
    "v_permlane32_swap_b32 v16, v18\n"
    "v_mfma_f32_16x16x32_bf16 v[116:119], v[236:239], v[80:83], v[116:119]\n"
    "v_permlane32_swap_b32 v17, v19\n"
    "s_nop 1\n"
    "v_permlane16_swap_b32 v16, v17\n"
    "v_permlane16_swap_b32 v18, v19\n"
    "v_mfma_f32_16x16x32_bf16 v[104:107], v[240:243], v[84:87], v[104:107]\n"
    "v_cvt_pk_bf16_f32 v20, v16, v17\n"
    "v_cvt_pk_bf16_f32 v21, v18, v19\n"
    "v_lshlrev_b32 v16, 16, v20\n"
    "v_mfma_f32_16x16x32_bf16 v[108:111], v[244:247], v[84:87], v[108:111]\n"
    "v_and_b32 v17, s60, v20\n"
    "v_lshlrev_b32 v18, 16, v21\n"
    "v_and_b32 v19, s60, v21\n"
    "v_mfma_f32_16x16x32_bf16 v[112:115], v[248:251], v[84:87], v[112:115]\n"
    "v_add_f32 v52, v52, v16\n"
    "v_add_f32 v53, v53, v17\n"
    "v_add_f32 v54, v54, v18\n"
    "v_mfma_f32_16x16x32_bf16 v[116:119], v[252:255], v[84:87], v[116:119]\n"
    "v_add_f32 v55, v55, v19\n"
    "v_cvt_pk_bf16_f32 v56, v52, v53\n"
    "v_cvt_pk_bf16_f32 v57, v54, v55\n"
    "global_store_dwordx2 v9, v[56:57], s[26:27] sc1 nt\n"
    "v_add_u32 v7, 69632, v7\n"
    "v_add_u32 v9, 1048576, v9\n"
    "ds_write_b128 v14, v[104:107] offset:0\n"
    "ds_write_b128 v14, v[108:111] offset:64\n"
    "ds_write_b128 v14, v[112:115] offset:128\n"
    "ds_write_b128 v14, v[116:119] offset:192\n"
    "s_waitcnt lgkmcnt(0)\n"
    "s_barrier\n"
    "ds_read_b128 v[60:63], v7 offset:0\n"
    "ds_read_b128 v[64:67], v7 offset:8704\n"
    "ds_read_b128 v[68:71], v7 offset:17408\n"
    "ds_read_b128 v[72:75], v7 offset:26112\n"
    "ds_read_b128 v[76:79], v7 offset:34816\n"
    "ds_read_b128 v[80:83], v7 offset:43520\n"
    "ds_read_b128 v[84:87], v7 offset:52224\n"
    "ds_read_b128 v[16:19], v7 offset:60928\n"
    "v_lshlrev_b32 v52, 16, v50\n"
    "v_and_b32 v53, s60, v50\n"
    "v_lshlrev_b32 v54, 16, v51\n"
    "v_and_b32 v55, s60, v51\n"
    "s_waitcnt lgkmcnt(6)\n"
    "v_pk_add_f32 v[60:61], v[60:61], v[64:65]\n"
    "v_pk_add_f32 v[62:63], v[62:63], v[66:67]\n"
    "s_waitcnt lgkmcnt(4)\n"
    "v_pk_add_f32 v[68:69], v[68:69], v[72:73]\n"
    "v_pk_add_f32 v[70:71], v[70:71], v[74:75]\n"
    "v_pk_add_f32 v[60:61], v[60:61], v[68:69]\n"
    "v_pk_add_f32 v[62:63], v[62:63], v[70:71]\n"
    "s_waitcnt lgkmcnt(2)\n"
    "v_pk_add_f32 v[76:77], v[76:77], v[80:81]\n"
    "v_pk_add_f32 v[78:79], v[78:79], v[82:83]\n"
    "s_waitcnt lgkmcnt(0)\n"
    "v_pk_add_f32 v[84:85], v[84:85], v[16:17]\n"
    "v_pk_add_f32 v[86:87], v[86:87], v[18:19]\n"
    "v_pk_add_f32 v[76:77], v[76:77], v[84:85]\n"
    "v_pk_add_f32 v[78:79], v[78:79], v[86:87]\n"
    "v_pk_add_f32 v[60:61], v[60:61], v[76:77]\n"
    "v_pk_add_f32 v[62:63], v[62:63], v[78:79]\n"
    "s_nop 1\n"
    "v_permlane32_swap_b32 v60, v62\n"
    "v_permlane32_swap_b32 v61, v63\n"
    "s_nop 1\n"
    "v_permlane16_swap_b32 v60, v61\n"
    "v_permlane16_swap_b32 v62, v63\n"
    "v_cvt_pk_bf16_f32 v64, v60, v61\n"
    "v_cvt_pk_bf16_f32 v65, v62, v63\n"
    "v_lshlrev_b32 v60, 16, v64\n"
    "v_and_b32 v61, s60, v64\n"
    "v_lshlrev_b32 v62, 16, v65\n"
    "v_and_b32 v63, s60, v65\n"
    "v_add_f32 v52, v52, v60\n"
    "v_add_f32 v53, v53, v61\n"
    "v_add_f32 v54, v54, v62\n"
    "v_add_f32 v55, v55, v63\n"
    "v_cvt_pk_bf16_f32 v56, v52, v53\n"
    "v_cvt_pk_bf16_f32 v57, v54, v55\n"
    "global_store_dwordx2 v9, v[56:57], s[26:27] sc1 nt\n"
    "s_endpgm\n"
    ".Lshuf0:\n"
    "s_mov_b32 vcc_lo, 0xff00ff00\n"
    "s_mov_b32 vcc_hi, 0xff00ff00\n"
    "v_perm_b32 v120, v150, v146, s41\n"
    "v_perm_b32 v121, v158, v154, s41\n"
    "v_perm_b32 v122, v150, v146, s42\n"
    "v_perm_b32 v123, v158, v154, s42\n"
    "v_perm_b32 v124, v151, v147, s41\n"
    "v_perm_b32 v125, v159, v155, s41\n"
    "v_perm_b32 v126, v151, v147, s42\n"
    "v_perm_b32 v127, v159, v155, s42\n"
    "v_perm_b32 v146, v148, v144, s41\n"
    "v_perm_b32 v147, v156, v152, s41\n"
    "v_perm_b32 v150, v148, v144, s42\n"
    "v_perm_b32 v151, v156, v152, s42\n"
    "v_perm_b32 v154, v149, v145, s41\n"
    "v_perm_b32 v155, v157, v153, s41\n"
    "v_perm_b32 v158, v149, v145, s42\n"
    "v_perm_b32 v159, v157, v153, s42\n"
    "s_not_b64 vcc, vcc\n"
    "v_cndmask_b32_dpp v144, v120, v146, vcc row_ror:8 row_mask:0xf bank_mask:0xf\n"
    "v_cndmask_b32_dpp v145, v121, v147, vcc row_ror:8 row_mask:0xf bank_mask:0xf\n"
    "v_cndmask_b32_dpp v148, v122, v150, vcc row_ror:8 row_mask:0xf bank_mask:0xf\n"
    "v_cndmask_b32_dpp v149, v123, v151, vcc row_ror:8 row_mask:0xf bank_mask:0xf\n"
    "v_cndmask_b32_dpp v152, v124, v154, vcc row_ror:8 row_mask:0xf bank_mask:0xf\n"
    "v_cndmask_b32_dpp v153, v125, v155, vcc row_ror:8 row_mask:0xf bank_mask:0xf\n"
    "v_cndmask_b32_dpp v156, v126, v158, vcc row_ror:8 row_mask:0xf bank_mask:0xf\n"
    "v_cndmask_b32_dpp v157, v127, v159, vcc row_ror:8 row_mask:0xf bank_mask:0xf\n"
    "s_not_b64 vcc, vcc\n"
    "v_cndmask_b32_dpp v146, v146, v120, vcc row_ror:8 row_mask:0xf bank_mask:0xf\n"
    "v_cndmask_b32_dpp v147, v147, v121, vcc row_ror:8 row_mask:0xf bank_mask:0xf\n"
    "v_cndmask_b32_dpp v150, v150, v122, vcc row_ror:8 row_mask:0xf bank_mask:0xf\n"
    "v_cndmask_b32_dpp v151, v151, v123, vcc row_ror:8 row_mask:0xf bank_mask:0xf\n"
    "v_cndmask_b32_dpp v154, v154, v124, vcc row_ror:8 row_mask:0xf bank_mask:0xf\n"
    "v_cndmask_b32_dpp v155, v155, v125, vcc row_ror:8 row_mask:0xf bank_mask:0xf\n"
    "v_cndmask_b32_dpp v158, v158, v126, vcc row_ror:8 row_mask:0xf bank_mask:0xf\n"
    "v_cndmask_b32_dpp v159, v159, v127, vcc row_ror:8 row_mask:0xf bank_mask:0xf\n"
    "s_nop 1\n"
    "s_setpc_b64 s[56:57]\n"
    ".Lshuf1:\n"
    "s_mov_b32 vcc_lo, 0xff00ff00\n"
    "s_mov_b32 vcc_hi, 0xff00ff00\n"
    "v_perm_b32 v128, v166, v162, s41\n"
    "v_perm_b32 v129, v174, v170, s41\n"
    "v_perm_b32 v130, v166, v162, s42\n"
    "v_perm_b32 v131, v174, v170, s42\n"
    "v_perm_b32 v132, v167, v163, s41\n"
    "v_perm_b32 v133, v175, v171, s41\n"
    "v_perm_b32 v134, v167, v163, s42\n"
    "v_perm_b32 v135, v175, v171, s42\n"
    "v_perm_b32 v162, v164, v160, s41\n"
    "v_perm_b32 v163, v172, v168, s41\n"
    "v_perm_b32 v166, v164, v160, s42\n"
    "v_perm_b32 v167, v172, v168, s42\n"
    "v_perm_b32 v170, v165, v161, s41\n"
    "v_perm_b32 v171, v173, v169, s41\n"
    "v_perm_b32 v174, v165, v161, s42\n"
    "v_perm_b32 v175, v173, v169, s42\n"
    "s_not_b64 vcc, vcc\n"
    "v_cndmask_b32_dpp v160, v128, v162, vcc row_ror:8 row_mask:0xf bank_mask:0xf\n"
    "v_cndmask_b32_dpp v161, v129, v163, vcc row_ror:8 row_mask:0xf bank_mask:0xf\n"
    "v_cndmask_b32_dpp v164, v130, v166, vcc row_ror:8 row_mask:0xf bank_mask:0xf\n"
    "v_cndmask_b32_dpp v165, v131, v167, vcc row_ror:8 row_mask:0xf bank_mask:0xf\n"
    "v_cndmask_b32_dpp v168, v132, v170, vcc row_ror:8 row_mask:0xf bank_mask:0xf\n"
    "v_cndmask_b32_dpp v169, v133, v171, vcc row_ror:8 row_mask:0xf bank_mask:0xf\n"
    "v_cndmask_b32_dpp v172, v134, v174, vcc row_ror:8 row_mask:0xf bank_mask:0xf\n"
    "v_cndmask_b32_dpp v173, v135, v175, vcc row_ror:8 row_mask:0xf bank_mask:0xf\n"
    "s_not_b64 vcc, vcc\n"
    "v_cndmask_b32_dpp v162, v162, v128, vcc row_ror:8 row_mask:0xf bank_mask:0xf\n"
    "v_cndmask_b32_dpp v163, v163, v129, vcc row_ror:8 row_mask:0xf bank_mask:0xf\n"
    "v_cndmask_b32_dpp v166, v166, v130, vcc row_ror:8 row_mask:0xf bank_mask:0xf\n"
    "v_cndmask_b32_dpp v167, v167, v131, vcc row_ror:8 row_mask:0xf bank_mask:0xf\n"
    "v_cndmask_b32_dpp v170, v170, v132, vcc row_ror:8 row_mask:0xf bank_mask:0xf\n"
    "v_cndmask_b32_dpp v171, v171, v133, vcc row_ror:8 row_mask:0xf bank_mask:0xf\n"
    "v_cndmask_b32_dpp v174, v174, v134, vcc row_ror:8 row_mask:0xf bank_mask:0xf\n"
    "v_cndmask_b32_dpp v175, v175, v135, vcc row_ror:8 row_mask:0xf bank_mask:0xf\n"
    "s_nop 1\n"
    "s_setpc_b64 s[56:57]\n"
    ".Lshuf2:\n"
    "s_mov_b32 vcc_lo, 0xff00ff00\n"
    "s_mov_b32 vcc_hi, 0xff00ff00\n"
    "v_perm_b32 v120, v182, v178, s41\n"
    "v_perm_b32 v121, v190, v186, s41\n"
    "v_perm_b32 v122, v182, v178, s42\n"
    "v_perm_b32 v123, v190, v186, s42\n"
    "v_perm_b32 v124, v183, v179, s41\n"
    "v_perm_b32 v125, v191, v187, s41\n"
    "v_perm_b32 v126, v183, v179, s42\n"
    "v_perm_b32 v127, v191, v187, s42\n"
    "v_perm_b32 v178, v180, v176, s41\n"
    "v_perm_b32 v179, v188, v184, s41\n"
    "v_perm_b32 v182, v180, v176, s42\n"
    "v_perm_b32 v183, v188, v184, s42\n"
    "v_perm_b32 v186, v181, v177, s41\n"
    "v_perm_b32 v187, v189, v185, s41\n"
    "v_perm_b32 v190, v181, v177, s42\n"
    "v_perm_b32 v191, v189, v185, s42\n"
    "s_not_b64 vcc, vcc\n"
    "v_cndmask_b32_dpp v176, v120, v178, vcc row_ror:8 row_mask:0xf bank_mask:0xf\n"
    "v_cndmask_b32_dpp v177, v121, v179, vcc row_ror:8 row_mask:0xf bank_mask:0xf\n"
    "v_cndmask_b32_dpp v180, v122, v182, vcc row_ror:8 row_mask:0xf bank_mask:0xf\n"
    "v_cndmask_b32_dpp v181, v123, v183, vcc row_ror:8 row_mask:0xf bank_mask:0xf\n"
    "v_cndmask_b32_dpp v184, v124, v186, vcc row_ror:8 row_mask:0xf bank_mask:0xf\n"
    "v_cndmask_b32_dpp v185, v125, v187, vcc row_ror:8 row_mask:0xf bank_mask:0xf\n"
    "v_cndmask_b32_dpp v188, v126, v190, vcc row_ror:8 row_mask:0xf bank_mask:0xf\n"
    "v_cndmask_b32_dpp v189, v127, v191, vcc row_ror:8 row_mask:0xf bank_mask:0xf\n"
    "s_not_b64 vcc, vcc\n"
    "v_cndmask_b32_dpp v178, v178, v120, vcc row_ror:8 row_mask:0xf bank_mask:0xf\n"
    "v_cndmask_b32_dpp v179, v179, v121, vcc row_ror:8 row_mask:0xf bank_mask:0xf\n"
    "v_cndmask_b32_dpp v182, v182, v122, vcc row_ror:8 row_mask:0xf bank_mask:0xf\n"
    "v_cndmask_b32_dpp v183, v183, v123, vcc row_ror:8 row_mask:0xf bank_mask:0xf\n"
    "v_cndmask_b32_dpp v186, v186, v124, vcc row_ror:8 row_mask:0xf bank_mask:0xf\n"
    "v_cndmask_b32_dpp v187, v187, v125, vcc row_ror:8 row_mask:0xf bank_mask:0xf\n"
    "v_cndmask_b32_dpp v190, v190, v126, vcc row_ror:8 row_mask:0xf bank_mask:0xf\n"
    "v_cndmask_b32_dpp v191, v191, v127, vcc row_ror:8 row_mask:0xf bank_mask:0xf\n"
    "s_nop 1\n"
    "s_setpc_b64 s[56:57]\n"
    ".Lshuf3:\n"
    "s_mov_b32 vcc_lo, 0xff00ff00\n"
    "s_mov_b32 vcc_hi, 0xff00ff00\n"
    "v_perm_b32 v128, v198, v194, s41\n"
    "v_perm_b32 v129, v206, v202, s41\n"
    "v_perm_b32 v130, v198, v194, s42\n"
    "v_perm_b32 v131, v206, v202, s42\n"
    "v_perm_b32 v132, v199, v195, s41\n"
    "v_perm_b32 v133, v207, v203, s41\n"
    "v_perm_b32 v134, v199, v195, s42\n"
    "v_perm_b32 v135, v207, v203, s42\n"
    "v_perm_b32 v194, v196, v192, s41\n"
    "v_perm_b32 v195, v204, v200, s41\n"
    "v_perm_b32 v198, v196, v192, s42\n"
    "v_perm_b32 v199, v204, v200, s42\n"
    "v_perm_b32 v202, v197, v193, s41\n"
    "v_perm_b32 v203, v205, v201, s41\n"
    "v_perm_b32 v206, v197, v193, s42\n"
    "v_perm_b32 v207, v205, v201, s42\n"
    "s_not_b64 vcc, vcc\n"
    "v_cndmask_b32_dpp v192, v128, v194, vcc row_ror:8 row_mask:0xf bank_mask:0xf\n"
    "v_cndmask_b32_dpp v193, v129, v195, vcc row_ror:8 row_mask:0xf bank_mask:0xf\n"
    "v_cndmask_b32_dpp v196, v130, v198, vcc row_ror:8 row_mask:0xf bank_mask:0xf\n"
    "v_cndmask_b32_dpp v197, v131, v199, vcc row_ror:8 row_mask:0xf bank_mask:0xf\n"
    "v_cndmask_b32_dpp v200, v132, v202, vcc row_ror:8 row_mask:0xf bank_mask:0xf\n"
    "v_cndmask_b32_dpp v201, v133, v203, vcc row_ror:8 row_mask:0xf bank_mask:0xf\n"
    "v_cndmask_b32_dpp v204, v134, v206, vcc row_ror:8 row_mask:0xf bank_mask:0xf\n"
    "v_cndmask_b32_dpp v205, v135, v207, vcc row_ror:8 row_mask:0xf bank_mask:0xf\n"
    "s_not_b64 vcc, vcc\n"
    "v_cndmask_b32_dpp v194, v194, v128, vcc row_ror:8 row_mask:0xf bank_mask:0xf\n"
    "v_cndmask_b32_dpp v195, v195, v129, vcc row_ror:8 row_mask:0xf bank_mask:0xf\n"
    "v_cndmask_b32_dpp v198, v198, v130, vcc row_ror:8 row_mask:0xf bank_mask:0xf\n"
    "v_cndmask_b32_dpp v199, v199, v131, vcc row_ror:8 row_mask:0xf bank_mask:0xf\n"
    "v_cndmask_b32_dpp v202, v202, v132, vcc row_ror:8 row_mask:0xf bank_mask:0xf\n"
    "v_cndmask_b32_dpp v203, v203, v133, vcc row_ror:8 row_mask:0xf bank_mask:0xf\n"
    "v_cndmask_b32_dpp v206, v206, v134, vcc row_ror:8 row_mask:0xf bank_mask:0xf\n"
    "v_cndmask_b32_dpp v207, v207, v135, vcc row_ror:8 row_mask:0xf bank_mask:0xf\n"
    "s_nop 1\n"
    "s_setpc_b64 s[56:57]\n"
    ".Lshuf4:\n"
    "s_mov_b32 vcc_lo, 0xff00ff00\n"
    "s_mov_b32 vcc_hi, 0xff00ff00\n"
    "v_perm_b32 v120, v214, v210, s41\n"
    "v_perm_b32 v121, v222, v218, s41\n"
    "v_perm_b32 v122, v214, v210, s42\n"
    "v_perm_b32 v123, v222, v218, s42\n"
    "v_perm_b32 v124, v215, v211, s41\n"
    "v_perm_b32 v125, v223, v219, s41\n"
    "v_perm_b32 v126, v215, v211, s42\n"
    "v_perm_b32 v127, v223, v219, s42\n"
    "v_perm_b32 v210, v212, v208, s41\n"
    "v_perm_b32 v211, v220, v216, s41\n"
    "v_perm_b32 v214, v212, v208, s42\n"
    "v_perm_b32 v215, v220, v216, s42\n"
    "v_perm_b32 v218, v213, v209, s41\n"
    "v_perm_b32 v219, v221, v217, s41\n"
    "v_perm_b32 v222, v213, v209, s42\n"
    "v_perm_b32 v223, v221, v217, s42\n"
    "s_not_b64 vcc, vcc\n"
    "v_cndmask_b32_dpp v208, v120, v210, vcc row_ror:8 row_mask:0xf bank_mask:0xf\n"
    "v_cndmask_b32_dpp v209, v121, v211, vcc row_ror:8 row_mask:0xf bank_mask:0xf\n"
    "v_cndmask_b32_dpp v212, v122, v214, vcc row_ror:8 row_mask:0xf bank_mask:0xf\n"
    "v_cndmask_b32_dpp v213, v123, v215, vcc row_ror:8 row_mask:0xf bank_mask:0xf\n"
    "v_cndmask_b32_dpp v216, v124, v218, vcc row_ror:8 row_mask:0xf bank_mask:0xf\n"
    "v_cndmask_b32_dpp v217, v125, v219, vcc row_ror:8 row_mask:0xf bank_mask:0xf\n"
    "v_cndmask_b32_dpp v220, v126, v222, vcc row_ror:8 row_mask:0xf bank_mask:0xf\n"
    "v_cndmask_b32_dpp v221, v127, v223, vcc row_ror:8 row_mask:0xf bank_mask:0xf\n"
    "s_not_b64 vcc, vcc\n"
    "v_cndmask_b32_dpp v210, v210, v120, vcc row_ror:8 row_mask:0xf bank_mask:0xf\n"
    "v_cndmask_b32_dpp v211, v211, v121, vcc row_ror:8 row_mask:0xf bank_mask:0xf\n"
    "v_cndmask_b32_dpp v214, v214, v122, vcc row_ror:8 row_mask:0xf bank_mask:0xf\n"
    "v_cndmask_b32_dpp v215, v215, v123, vcc row_ror:8 row_mask:0xf bank_mask:0xf\n"
    "v_cndmask_b32_dpp v218, v218, v124, vcc row_ror:8 row_mask:0xf bank_mask:0xf\n"
    "v_cndmask_b32_dpp v219, v219, v125, vcc row_ror:8 row_mask:0xf bank_mask:0xf\n"
    "v_cndmask_b32_dpp v222, v222, v126, vcc row_ror:8 row_mask:0xf bank_mask:0xf\n"
    "v_cndmask_b32_dpp v223, v223, v127, vcc row_ror:8 row_mask:0xf bank_mask:0xf\n"
    "s_nop 1\n"
    "s_setpc_b64 s[56:57]\n"
    ".Lshuf5:\n"
    "s_mov_b32 vcc_lo, 0xff00ff00\n"
    "s_mov_b32 vcc_hi, 0xff00ff00\n"
    "v_perm_b32 v128, v230, v226, s41\n"
    "v_perm_b32 v129, v238, v234, s41\n"
    "v_perm_b32 v130, v230, v226, s42\n"
    "v_perm_b32 v131, v238, v234, s42\n"
    "v_perm_b32 v132, v231, v227, s41\n"
    "v_perm_b32 v133, v239, v235, s41\n"
    "v_perm_b32 v134, v231, v227, s42\n"
    "v_perm_b32 v135, v239, v235, s42\n"
    "v_perm_b32 v226, v228, v224, s41\n"
    "v_perm_b32 v227, v236, v232, s41\n"
    "v_perm_b32 v230, v228, v224, s42\n"
    "v_perm_b32 v231, v236, v232, s42\n"
    "v_perm_b32 v234, v229, v225, s41\n"
    "v_perm_b32 v235, v237, v233, s41\n"
    "v_perm_b32 v238, v229, v225, s42\n"
    "v_perm_b32 v239, v237, v233, s42\n"
    "s_not_b64 vcc, vcc\n"
    "v_cndmask_b32_dpp v224, v128, v226, vcc row_ror:8 row_mask:0xf bank_mask:0xf\n"
    "v_cndmask_b32_dpp v225, v129, v227, vcc row_ror:8 row_mask:0xf bank_mask:0xf\n"
    "v_cndmask_b32_dpp v228, v130, v230, vcc row_ror:8 row_mask:0xf bank_mask:0xf\n"
    "v_cndmask_b32_dpp v229, v131, v231, vcc row_ror:8 row_mask:0xf bank_mask:0xf\n"
    "v_cndmask_b32_dpp v232, v132, v234, vcc row_ror:8 row_mask:0xf bank_mask:0xf\n"
    "v_cndmask_b32_dpp v233, v133, v235, vcc row_ror:8 row_mask:0xf bank_mask:0xf\n"
    "v_cndmask_b32_dpp v236, v134, v238, vcc row_ror:8 row_mask:0xf bank_mask:0xf\n"
    "v_cndmask_b32_dpp v237, v135, v239, vcc row_ror:8 row_mask:0xf bank_mask:0xf\n"
    "s_not_b64 vcc, vcc\n"
    "v_cndmask_b32_dpp v226, v226, v128, vcc row_ror:8 row_mask:0xf bank_mask:0xf\n"
    "v_cndmask_b32_dpp v227, v227, v129, vcc row_ror:8 row_mask:0xf bank_mask:0xf\n"
    "v_cndmask_b32_dpp v230, v230, v130, vcc row_ror:8 row_mask:0xf bank_mask:0xf\n"
    "v_cndmask_b32_dpp v231, v231, v131, vcc row_ror:8 row_mask:0xf bank_mask:0xf\n"
    "v_cndmask_b32_dpp v234, v234, v132, vcc row_ror:8 row_mask:0xf bank_mask:0xf\n"
    "v_cndmask_b32_dpp v235, v235, v133, vcc row_ror:8 row_mask:0xf bank_mask:0xf\n"
    "v_cndmask_b32_dpp v238, v238, v134, vcc row_ror:8 row_mask:0xf bank_mask:0xf\n"
    "v_cndmask_b32_dpp v239, v239, v135, vcc row_ror:8 row_mask:0xf bank_mask:0xf\n"
    "s_nop 1\n"
    "s_setpc_b64 s[56:57]\n"
    ".Lshuf6:\n"
    "s_mov_b32 vcc_lo, 0xff00ff00\n"
    "s_mov_b32 vcc_hi, 0xff00ff00\n"
    "v_perm_b32 v120, v246, v242, s41\n"
    "v_perm_b32 v121, v254, v250, s41\n"
    "v_perm_b32 v122, v246, v242, s42\n"
    "v_perm_b32 v123, v254, v250, s42\n"
    "v_perm_b32 v124, v247, v243, s41\n"
    "v_perm_b32 v125, v255, v251, s41\n"
    "v_perm_b32 v126, v247, v243, s42\n"
    "v_perm_b32 v127, v255, v251, s42\n"
    "v_perm_b32 v242, v244, v240, s41\n"
    "v_perm_b32 v243, v252, v248, s41\n"
    "v_perm_b32 v246, v244, v240, s42\n"
    "v_perm_b32 v247, v252, v248, s42\n"
    "v_perm_b32 v250, v245, v241, s41\n"
    "v_perm_b32 v251, v253, v249, s41\n"
    "v_perm_b32 v254, v245, v241, s42\n"
    "v_perm_b32 v255, v253, v249, s42\n"
    "s_not_b64 vcc, vcc\n"
    "v_cndmask_b32_dpp v240, v120, v242, vcc row_ror:8 row_mask:0xf bank_mask:0xf\n"
    "v_cndmask_b32_dpp v241, v121, v243, vcc row_ror:8 row_mask:0xf bank_mask:0xf\n"
    "v_cndmask_b32_dpp v244, v122, v246, vcc row_ror:8 row_mask:0xf bank_mask:0xf\n"
    "v_cndmask_b32_dpp v245, v123, v247, vcc row_ror:8 row_mask:0xf bank_mask:0xf\n"
    "v_cndmask_b32_dpp v248, v124, v250, vcc row_ror:8 row_mask:0xf bank_mask:0xf\n"
    "v_cndmask_b32_dpp v249, v125, v251, vcc row_ror:8 row_mask:0xf bank_mask:0xf\n"
    "v_cndmask_b32_dpp v252, v126, v254, vcc row_ror:8 row_mask:0xf bank_mask:0xf\n"
    "v_cndmask_b32_dpp v253, v127, v255, vcc row_ror:8 row_mask:0xf bank_mask:0xf\n"
    "s_not_b64 vcc, vcc\n"
    "v_cndmask_b32_dpp v242, v242, v120, vcc row_ror:8 row_mask:0xf bank_mask:0xf\n"
    "v_cndmask_b32_dpp v243, v243, v121, vcc row_ror:8 row_mask:0xf bank_mask:0xf\n"
    "v_cndmask_b32_dpp v246, v246, v122, vcc row_ror:8 row_mask:0xf bank_mask:0xf\n"
    "v_cndmask_b32_dpp v247, v247, v123, vcc row_ror:8 row_mask:0xf bank_mask:0xf\n"
    "v_cndmask_b32_dpp v250, v250, v124, vcc row_ror:8 row_mask:0xf bank_mask:0xf\n"
    "v_cndmask_b32_dpp v251, v251, v125, vcc row_ror:8 row_mask:0xf bank_mask:0xf\n"
    "v_cndmask_b32_dpp v254, v254, v126, vcc row_ror:8 row_mask:0xf bank_mask:0xf\n"
    "v_cndmask_b32_dpp v255, v255, v127, vcc row_ror:8 row_mask:0xf bank_mask:0xf\n"
    "s_nop 1\n"
    "s_setpc_b64 s[56:57]\n"
    ".LsubQ:\n"
    "buffer_load_dwordx4 v[128:131], v1, s[4:7], s44 offen offset:1024 sc1\n"
    "buffer_load_dwordx4 v[132:135], v1, s[4:7], s44 offen offset:2048 sc1\n"
    "buffer_load_dwordx4 v[136:139], v11, s[4:7], s14 offen sc1\n"
    "buffer_load_dwordx4 v[140:143], v11, s[4:7], s14 offen offset:1024 sc1\n"
    ".LsubQ0:\n"
    "s_waitcnt vmcnt(0)\n"
    "v_xor_b32 v131, s37, v131\n"
    "v_xor_b32 v135, s37, v135\n"
    "v_or_b32 v12, v131, v135\n"
    "v_cmp_ne_u32_e32 vcc, 0, v12\n"
    "s_cbranch_vccz .LsubQ0d\n"
    "s_sleep 2\n"
    "buffer_load_dwordx4 v[128:131], v1, s[4:7], s44 offen offset:1024 sc1\n"
    "buffer_load_dwordx4 v[132:135], v1, s[4:7], s44 offen offset:2048 sc1\n"
    "s_add_u32 s40, s40, 1\n"
    "s_cmp_lt_u32 s40, 262144\n"
    "s_cbranch_scc1 .LsubQ0\n"
    "s_waitcnt vmcnt(0)\n"
    ".LsubQ0d:\n"
    ".LsubQ1:\n"
    "s_waitcnt vmcnt(0)\n"
    "v_xor_b32 v139, s37, v139\n"
    "v_xor_b32 v143, s37, v143\n"
    "v_or_b32 v12, v139, v143\n"
    "v_max_u32 v138, v138, v142\n"
    "v_cmp_ne_u32_e32 vcc, 0, v12\n"
    "s_cbranch_vccz .LsubQ1d\n"
    "s_sleep 2\n"
    "buffer_load_dwordx4 v[136:139], v11, s[4:7], s14 offen sc1\n"
    "buffer_load_dwordx4 v[140:143], v11, s[4:7], s14 offen offset:1024 sc1\n"
    "s_add_u32 s40, s40, 1\n"
    "s_cmp_lt_u32 s40, 262144\n"
    "s_cbranch_scc1 .LsubQ1\n"
    "s_waitcnt vmcnt(0)\n"
    ".LsubQ1d:\n"
    "ds_max_u32 v2, v138 offset:4\n"
    "s_mov_b64 exec, 1\n"
    "ds_inc_u32 v2, v2 offset:16\n"
    "s_mov_b64 exec, -1\n"
    "v_and_b32 v3, s60, v137\n"
    "v_rcp_f32 v3, v3\n"
    "v_lshlrev_b32 v12, 16, v137\n"
    "v_lshlrev_b32 v131, 16, v129\n"
    "v_mul_f32 v12, v12, v3\n"
    "v_and_b32 v129, s60, v129\n"
    "v_fma_f32 v138, v131, v3, -v12\n"
    "v_fma_f32 v139, v129, v3, -v12\n"
    "v_and_b32 v3, s60, v136\n"
    "v_rcp_f32 v3, v3\n"
    "v_lshlrev_b32 v12, 16, v136\n"
    "v_lshlrev_b32 v130, 16, v128\n"
    "v_mul_f32 v12, v12, v3\n"
    "v_and_b32 v128, s60, v128\n"
    "v_fma_f32 v136, v130, v3, -v12\n"
    "v_fma_f32 v137, v128, v3, -v12\n"
    "v_and_b32 v3, s60, v141\n"
    "v_rcp_f32 v3, v3\n"
    "v_lshlrev_b32 v12, 16, v141\n"
    "v_lshlrev_b32 v135, 16, v133\n"
    "v_mul_f32 v12, v12, v3\n"
    "v_and_b32 v133, s60, v133\n"
    "v_fma_f32 v142, v135, v3, -v12\n"
    "v_fma_f32 v143, v133, v3, -v12\n"
    "v_and_b32 v3, s60, v140\n"
    "v_rcp_f32 v3, v3\n"
    "v_lshlrev_b32 v12, 16, v140\n"
    "v_lshlrev_b32 v134, 16, v132\n"
    "v_mul_f32 v12, v12, v3\n"
    "v_and_b32 v132, s60, v132\n"
    "v_fma_f32 v140, v134, v3, -v12\n"
    "v_fma_f32 v141, v132, v3, -v12\n"
    "s_setpc_b64 s[56:57]\n"
    ".Lrec_s0:\n"
    "s_sleep 2\n"
    "buffer_load_dwordx4 v[114:117], v1, s[4:7], s44 offen sc1\n"
    "buffer_load_dwordx2 v[118:119], v230, s[4:7], s44 offen offset:3072 sc1\n"
    "s_waitcnt vmcnt(0)\n"
    "v_xor_b32 v117, s37, v117\n"
    "v_xor_b32 v119, s37, v119\n"
    "v_or_b32 v117, v117, v119\n"
    "v_cmp_ne_u32_e32 vcc, 0, v117\n"
    "s_cbranch_vccz .Lrec_b0\n"
    "s_add_u32 s40, s40, 1\n"
    "s_cmp_lt_u32 s40, 262144\n"
    "s_cbranch_scc1 .Lrec_s0\n"
    "s_branch .Lrec_b0\n"
    ".Ls_3:\n"
    ".Lw_5:\n"
    "ds_read_b32 v3, v2 offset:20\n"
    "s_waitcnt lgkmcnt(0)\n"
    "v_readfirstlane_b32 s48, v3\n"
    "s_cmp_ge_u32 s48, 8\n"
    "s_cbranch_scc1 .Lb_4\n"
    "s_add_u32 s40, s40, 1\n"
    "s_cmp_lt_u32 s40, 262144\n"
    "s_cbranch_scc1 .Lw_5\n"
    "s_branch .Lb_4\n"
    ".Ls_6:\n"
    ".Lw_8:\n"
    "ds_read_b32 v3, v2 offset:32\n"
    "s_waitcnt lgkmcnt(0)\n"
    "v_readfirstlane_b32 s48, v3\n"
    "s_cmp_ge_u32 s48, 8\n"
    "s_cbranch_scc1 .Lb_7\n"
    "s_add_u32 s40, s40, 1\n"
    "s_cmp_lt_u32 s40, 262144\n"
    "s_cbranch_scc1 .Lw_8\n"
    "s_branch .Lb_7\n"
    ".Ls_10:\n"
    ".Lw_12:\n"
    "ds_read_b32 v3, v2 offset:28\n"
    "s_waitcnt lgkmcnt(0)\n"
    "v_readfirstlane_b32 s48, v3\n"
    "s_cmp_ge_u32 s48, 4\n"
    "s_cbranch_scc1 .Lb_11\n"
    "s_add_u32 s40, s40, 1\n"
    "s_cmp_lt_u32 s40, 262144\n"
    "s_cbranch_scc1 .Lw_12\n"
    "s_branch .Lb_11\n"
    ".Ls_13:\n"
    ".Lw_15:\n"
    "ds_read_b32 v3, v2 offset:16\n"
    "s_waitcnt lgkmcnt(0)\n"
    "v_readfirstlane_b32 s48, v3\n"
    "s_cmp_ge_u32 s48, 8\n"
    "s_cbranch_scc1 .Lb_14\n"
    "s_add_u32 s40, s40, 1\n"
    "s_cmp_lt_u32 s40, 262144\n"
    "s_cbranch_scc1 .Lw_15\n"
    "s_branch .Lb_14\n"
    ".Ls_16:\n"
    ".Lw_18:\n"
    "ds_read_b32 v3, v2 offset:36\n"
    "s_waitcnt lgkmcnt(0)\n"
    "v_readfirstlane_b32 s48, v3\n"
    "s_cmp_ge_u32 s48, 4\n"
    "s_cbranch_scc1 .Lb_17\n"
    "s_add_u32 s40, s40, 1\n"
    "s_cmp_lt_u32 s40, 262144\n"
    "s_cbranch_scc1 .Lw_18\n"
    "s_branch .Lb_17\n"
    ".Ls_21:\n"
    ".Lw_23:\n"
    "ds_read_b32 v3, v2 offset:20\n"
    "s_waitcnt lgkmcnt(0)\n"
    "v_readfirstlane_b32 s48, v3\n"
    "s_cmp_ge_u32 s48, 8\n"
    "s_cbranch_scc1 .Lb_22\n"
    "s_add_u32 s40, s40, 1\n"
    "s_cmp_lt_u32 s40, 262144\n"
    "s_cbranch_scc1 .Lw_23\n"
    "s_branch .Lb_22\n"
    ".Lrec_s1:\n"
    "s_sleep 2\n"
    "buffer_load_dwordx4 v[224:227], v1, s[4:7], s44 offen sc1\n"
    "buffer_load_dwordx2 v[228:229], v230, s[4:7], s44 offen offset:3072 sc1\n"
    "s_waitcnt vmcnt(0)\n"
    "v_xor_b32 v227, s37, v227\n"
    "v_xor_b32 v229, s37, v229\n"
    "v_or_b32 v227, v227, v229\n"
    "v_cmp_ne_u32_e32 vcc, 0, v227\n"
    "s_cbranch_vccz .Lrec_b1\n"
    "s_add_u32 s40, s40, 1\n"
    "s_cmp_lt_u32 s40, 262144\n"
    "s_cbranch_scc1 .Lrec_s1\n"
    "s_branch .Lrec_b1\n"
    ".Ls_24:\n"
    ".Lw_26:\n"
    "ds_read_b32 v3, v2 offset:28\n"
    "s_waitcnt lgkmcnt(0)\n"
    "v_readfirstlane_b32 s48, v3\n"
    "s_cmp_ge_u32 s48, 4\n"
    "s_cbranch_scc1 .Lb_25\n"
    "s_add_u32 s40, s40, 1\n"
    "s_cmp_lt_u32 s40, 262144\n"
    "s_cbranch_scc1 .Lw_26\n"
    "s_branch .Lb_25\n"
    ".Ls_28:\n"
    ".Lw_30:\n"
    "ds_read_b32 v3, v2 offset:36\n"
    "s_waitcnt lgkmcnt(0)\n"
    "v_readfirstlane_b32 s48, v3\n"
    "s_cmp_ge_u32 s48, 4\n"
    "s_cbranch_scc1 .Lb_29\n"
    "s_add_u32 s40, s40, 1\n"
    "s_cmp_lt_u32 s40, 262144\n"
    "s_cbranch_scc1 .Lw_30\n"
    "s_branch .Lb_29\n"
    ".Ls_33:\n"
    ".Lw_35:\n"
    "ds_read_b32 v3, v2 offset:16\n"
    "s_waitcnt lgkmcnt(0)\n"
    "v_readfirstlane_b32 s48, v3\n"
    "s_cmp_ge_u32 s48, 8\n"
    "s_cbranch_scc1 .Lb_34\n"
    "s_add_u32 s40, s40, 1\n"
    "s_cmp_lt_u32 s40, 262144\n"
    "s_cbranch_scc1 .Lw_35\n"
    "s_branch .Lb_34\n"
    ".LsubC0:\n"
    "s_sleep 2\n"
    "s_waitcnt vmcnt(0)\n"
    "s_branch .LsubC0x\n"
    ".LsubC1:\n"
    "s_sleep 2\n"
    "s_waitcnt vmcnt(0)\n"
    "s_branch .LsubC1x\n"
    ".LsubC2:\n"
    "s_sleep 2\n"
    "s_waitcnt vmcnt(0)\n"
    "s_branch .LsubC2x\n"
    ".LsubC3:\n"
    "s_sleep 2\n"
    "s_waitcnt vmcnt(0)\n"
    "s_branch .LsubC3x\n"
    ".LsubC4:\n"
    "s_sleep 2\n"
    "s_waitcnt vmcnt(0)\n"
    "s_branch .LsubC4x\n"
    ".LsubC5:\n"
    "s_sleep 2\n"
    "s_waitcnt vmcnt(0)\n"
    "s_branch .LsubC5x\n"
    ".LsubC6:\n"
    "s_sleep 2\n"
    "s_waitcnt vmcnt(0)\n"
    "s_branch .LsubC6x\n"
    ".LsubC7:\n"
    "s_sleep 2\n"
    "s_waitcnt vmcnt(0)\n"
    "s_branch .LsubC7x\n"
    ".LsubC0x:\n"
    "v_max3_u32 v15, v16, v17, v18\n"
    "v_max3_u32 v15, v15, v19, v20\n"
    "v_max3_u32 v15, v15, v21, v22\n"
    "v_max_u32 v15, v15, v23\n"
    "v_cmp_eq_u32_e32 vcc, -1, v15\n"
    "s_cbranch_vccz .LsubC0y\n"
    "buffer_load_dwordx2 v[16:17], v9, s[4:7], s8 offen offset:0 sc1\n"
    "buffer_load_dwordx2 v[18:19], v9, s[4:7], s8 offen offset:1024 sc1\n"
    "buffer_load_dwordx2 v[20:21], v11, s[4:7], s8 offen offset:0 sc1\n"
    "buffer_load_dwordx2 v[22:23], v11, s[4:7], s8 offen offset:1024 sc1\n"
    ".LsubC0y:\n"
    ".LsubC1x:\n"
    "v_max3_u32 v15, v24, v25, v26\n"
    "v_max3_u32 v15, v15, v27, v28\n"
    "v_max3_u32 v15, v15, v29, v30\n"
    "v_max_u32 v15, v15, v31\n"
    "v_cmp_eq_u32_e32 vcc, -1, v15\n"
    "s_cbranch_vccz .LsubC1y\n"
    "buffer_load_dwordx2 v[24:25], v9, s[4:7], s9 offen offset:0 sc1\n"
    "buffer_load_dwordx2 v[26:27], v9, s[4:7], s9 offen offset:1024 sc1\n"
    "buffer_load_dwordx2 v[28:29], v11, s[4:7], s9 offen offset:0 sc1\n"
    "buffer_load_dwordx2 v[30:31], v11, s[4:7], s9 offen offset:1024 sc1\n"
    ".LsubC1y:\n"
    ".LsubC2x:\n"
    "v_max3_u32 v15, v32, v33, v34\n"
    "v_max3_u32 v15, v15, v35, v36\n"
    "v_max3_u32 v15, v15, v37, v38\n"
    "v_max_u32 v15, v15, v39\n"
    "v_cmp_eq_u32_e32 vcc, -1, v15\n"
    "s_cbranch_vccz .LsubC2y\n"
    "buffer_load_dwordx2 v[32:33], v9, s[4:7], s10 offen offset:0 sc1\n"
    "buffer_load_dwordx2 v[34:35], v9, s[4:7], s10 offen offset:1024 sc1\n"
    "buffer_load_dwordx2 v[36:37], v11, s[4:7], s10 offen offset:0 sc1\n"
    "buffer_load_dwordx2 v[38:39], v11, s[4:7], s10 offen offset:1024 sc1\n"
    ".LsubC2y:\n"
    ".LsubC3x:\n"
    "v_max3_u32 v15, v40, v41, v42\n"
    "v_max3_u32 v15, v15, v43, v44\n"
    "v_max3_u32 v15, v15, v45, v46\n"
    "v_max_u32 v15, v15, v47\n"
    "v_cmp_eq_u32_e32 vcc, -1, v15\n"
    "s_cbranch_vccz .LsubC3y\n"
    "buffer_load_dwordx2 v[40:41], v9, s[4:7], s11 offen offset:0 sc1\n"
    "buffer_load_dwordx2 v[42:43], v9, s[4:7], s11 offen offset:1024 sc1\n"
    "buffer_load_dwordx2 v[44:45], v11, s[4:7], s11 offen offset:0 sc1\n"
    "buffer_load_dwordx2 v[46:47], v11, s[4:7], s11 offen offset:1024 sc1\n"
    ".LsubC3y:\n"
    ".LsubC4x:\n"
    "v_max3_u32 v15, v224, v225, v226\n"
    "v_max3_u32 v15, v15, v227, v228\n"
    "v_max3_u32 v15, v15, v229, v230\n"
    "v_max_u32 v15, v15, v231\n"
    "v_cmp_eq_u32_e32 vcc, -1, v15\n"
    "s_cbranch_vccz .LsubC4y\n"
    "buffer_load_dwordx2 v[224:225], v9, s[4:7], s12 offen offset:0 sc1\n"
    "buffer_load_dwordx2 v[226:227], v9, s[4:7], s12 offen offset:1024 sc1\n"
    "buffer_load_dwordx2 v[228:229], v11, s[4:7], s12 offen offset:0 sc1\n"
    "buffer_load_dwordx2 v[230:231], v11, s[4:7], s12 offen offset:1024 sc1\n"
    ".LsubC4y:\n"
    ".LsubC5x:\n"
    "v_max3_u32 v15, v232, v233, v234\n"
    "v_max3_u32 v15, v15, v235, v236\n"
    "v_max3_u32 v15, v15, v237, v238\n"
    "v_max_u32 v15, v15, v239\n"
    "v_cmp_eq_u32_e32 vcc, -1, v15\n"
    "s_cbranch_vccz .LsubC5y\n"
    "buffer_load_dwordx2 v[232:233], v9, s[4:7], s13 offen offset:0 sc1\n"
    "buffer_load_dwordx2 v[234:235], v9, s[4:7], s13 offen offset:1024 sc1\n"
    "buffer_load_dwordx2 v[236:237], v11, s[4:7], s13 offen offset:0 sc1\n"
    "buffer_load_dwordx2 v[238:239], v11, s[4:7], s13 offen offset:1024 sc1\n"
    ".LsubC5y:\n"
    ".LsubC6x:\n"
    "v_max3_u32 v15, v240, v241, v242\n"
    "v_max3_u32 v15, v15, v243, v244\n"
    "v_max3_u32 v15, v15, v245, v246\n"
    "v_max_u32 v15, v15, v247\n"
    "v_cmp_eq_u32_e32 vcc, -1, v15\n"
    "s_cbranch_vccz .LsubC6y\n"
    "buffer_load_dwordx2 v[240:241], v9, s[4:7], s14 offen offset:0 sc1\n"
    "buffer_load_dwordx2 v[242:243], v9, s[4:7], s14 offen offset:1024 sc1\n"
    "buffer_load_dwordx2 v[244:245], v11, s[4:7], s14 offen offset:0 sc1\n"
    "buffer_load_dwordx2 v[246:247], v11, s[4:7], s14 offen offset:1024 sc1\n"
    ".LsubC6y:\n"
    ".LsubC7x:\n"
    "v_max3_u32 v15, v248, v249, v250\n"
    "v_max3_u32 v15, v15, v251, v252\n"
    "v_max3_u32 v15, v15, v253, v254\n"
    "v_max_u32 v15, v15, v255\n"
    "v_cmp_eq_u32_e32 vcc, -1, v15\n"
    "s_cbranch_vccz .LsubC7y\n"
    "buffer_load_dwordx2 v[248:249], v9, s[4:7], s15 offen offset:0 sc1\n"
    "buffer_load_dwordx2 v[250:251], v9, s[4:7], s15 offen offset:1024 sc1\n"
    "buffer_load_dwordx2 v[252:253], v11, s[4:7], s15 offen offset:0 sc1\n"
    "buffer_load_dwordx2 v[254:255], v11, s[4:7], s15 offen offset:1024 sc1\n"
    ".LsubC7y:\n"
    "s_waitcnt vmcnt(0)\n"
    "s_setpc_b64 s[56:57]\n"
    ".Ls_41:\n"
    "s_call_b64 s[56:57], .LsubC0\n"
    "v_max3_u32 v15, v16, v17, v18\n"
    "v_max3_u32 v15, v15, v19, v20\n"
    "v_max3_u32 v15, v15, v21, v22\n"
    "v_max_u32 v15, v15, v23\n"
    "v_cmp_eq_u32_e32 vcc, -1, v15\n"
    "s_cbranch_vccz .Lb_42\n"
    "s_add_u32 s40, s40, 1\n"
    "s_cmp_lt_u32 s40, 262144\n"
    "s_cbranch_scc1 .Ls_41\n"
    "s_branch .Lb_42\n"
    ".LsubS:\n"
    "s_sleep 2\n"
    "buffer_load_dwordx2 v[52:53], v10, s[4:7], s46 offen offset:0 sc1\n"
    "buffer_load_dwordx2 v[54:55], v10, s[4:7], s46 offen offset:512 sc1\n"
    "buffer_load_dwordx2 v[56:57], v10, s[4:7], s46 offen offset:1024 sc1\n"
    "buffer_load_dwordx2 v[58:59], v10, s[4:7], s46 offen offset:1536 sc1\n"
    "s_waitcnt vmcnt(0)\n"
    "s_setpc_b64 s[56:57]\n"
    ".Ls_43:\n"
    "s_call_b64 s[56:57], .LsubC1\n"
    "v_max3_u32 v15, v24, v25, v26\n"
    "v_max3_u32 v15, v15, v27, v28\n"
    "v_max3_u32 v15, v15, v29, v30\n"
    "v_max_u32 v15, v15, v31\n"
    "v_cmp_eq_u32_e32 vcc, -1, v15\n"
    "s_cbranch_vccz .Lb_44\n"
    "s_add_u32 s40, s40, 1\n"
    "s_cmp_lt_u32 s40, 262144\n"
    "s_cbranch_scc1 .Ls_43\n"
    "s_branch .Lb_44\n"
    ".Ls_45:\n"
    "s_call_b64 s[56:57], .LsubC2\n"
    "v_max3_u32 v15, v32, v33, v34\n"
    "v_max3_u32 v15, v15, v35, v36\n"
    "v_max3_u32 v15, v15, v37, v38\n"
    "v_max_u32 v15, v15, v39\n"
    "v_cmp_eq_u32_e32 vcc, -1, v15\n"
    "s_cbranch_vccz .Lb_46\n"
    "s_add_u32 s40, s40, 1\n"
    "s_cmp_lt_u32 s40, 262144\n"
    "s_cbranch_scc1 .Ls_45\n"
    "s_branch .Lb_46\n"
    ".Ls_47:\n"
    "s_call_b64 s[56:57], .LsubC3\n"
    "v_max3_u32 v15, v40, v41, v42\n"
    "v_max3_u32 v15, v15, v43, v44\n"
    "v_max3_u32 v15, v15, v45, v46\n"
    "v_max_u32 v15, v15, v47\n"
    "v_cmp_eq_u32_e32 vcc, -1, v15\n"
    "s_cbranch_vccz .Lb_48\n"
    "s_add_u32 s40, s40, 1\n"
    "s_cmp_lt_u32 s40, 262144\n"
    "s_cbranch_scc1 .Ls_47\n"
    "s_branch .Lb_48\n"
    ".Ls_49:\n"
    "s_call_b64 s[56:57], .LsubC4\n"
    "v_max3_u32 v15, v224, v225, v226\n"
    "v_max3_u32 v15, v15, v227, v228\n"
    "v_max3_u32 v15, v15, v229, v230\n"
    "v_max_u32 v15, v15, v231\n"
    "v_cmp_eq_u32_e32 vcc, -1, v15\n"
    "s_cbranch_vccz .Lb_50\n"
    "s_add_u32 s40, s40, 1\n"
    "s_cmp_lt_u32 s40, 262144\n"
    "s_cbranch_scc1 .Ls_49\n"
    "s_branch .Lb_50\n"
    ".Ls_51:\n"
    "s_call_b64 s[56:57], .LsubC5\n"
    "v_max3_u32 v15, v232, v233, v234\n"
    "v_max3_u32 v15, v15, v235, v236\n"
    "v_max3_u32 v15, v15, v237, v238\n"
    "v_max_u32 v15, v15, v239\n"
    "v_cmp_eq_u32_e32 vcc, -1, v15\n"
    "s_cbranch_vccz .Lb_52\n"
    "s_add_u32 s40, s40, 1\n"
    "s_cmp_lt_u32 s40, 262144\n"
    "s_cbranch_scc1 .Ls_51\n"
    "s_branch .Lb_52\n"
    ".Ls_53:\n"
    "s_call_b64 s[56:57], .LsubC6\n"
    "v_max3_u32 v15, v240, v241, v242\n"
    "v_max3_u32 v15, v15, v243, v244\n"
    "v_max3_u32 v15, v15, v245, v246\n"
    "v_max_u32 v15, v15, v247\n"
    "v_cmp_eq_u32_e32 vcc, -1, v15\n"
    "s_cbranch_vccz .Lb_54\n"
    "s_add_u32 s40, s40, 1\n"
    "s_cmp_lt_u32 s40, 262144\n"
    "s_cbranch_scc1 .Ls_53\n"
    "s_branch .Lb_54\n"
    ".Ls_55:\n"
    "s_call_b64 s[56:57], .LsubC7\n"
    "v_max3_u32 v15, v248, v249, v250\n"
    "v_max3_u32 v15, v15, v251, v252\n"
    "v_max3_u32 v15, v15, v253, v254\n"
    "v_max_u32 v15, v15, v255\n"
    "v_cmp_eq_u32_e32 vcc, -1, v15\n"
    "s_cbranch_vccz .Lb_56\n"
    "s_add_u32 s40, s40, 1\n"
    "s_cmp_lt_u32 s40, 262144\n"
    "s_cbranch_scc1 .Ls_55\n"
    "s_branch .Lb_56\n"
    ".Ls_57:\n"
    ".Lw_59:\n"
    "ds_read_b32 v3, v2 offset:52\n"
    "s_waitcnt lgkmcnt(0)\n"
    "v_readfirstlane_b32 s48, v3\n"
    "s_cmp_ge_u32 s48, 8\n"
    "s_cbranch_scc1 .Lb_58\n"
    "s_add_u32 s40, s40, 1\n"
    "s_cmp_lt_u32 s40, 262144\n"
    "s_cbranch_scc1 .Lw_59\n"
    "s_branch .Lb_58\n"
    ".Ls_60:\n"
    ".Lw_62:\n"
    "ds_read_b32 v3, v14 offset:36\n"
    "s_waitcnt lgkmcnt(0)\n"
    "v_readfirstlane_b32 s48, v3\n"
    "s_cmp_ge_u32 s48, 8\n"
    "s_cbranch_scc1 .Lb_61\n"
    "s_add_u32 s40, s40, 1\n"
    "s_cmp_lt_u32 s40, 262144\n"
    "s_cbranch_scc1 .Lw_62\n"
    "s_branch .Lb_61\n"
    ".Ls_63:\n"
    "s_call_b64 s[56:57], .LsubS\n"
    "v_max3_u32 v15, v53, v55, v57\n"
    "v_max_u32 v15, v15, v59\n"
    "v_cmp_eq_u32_e32 vcc, -1, v15\n"
    "s_cbranch_vccz .Lb_64\n"
    "s_add_u32 s40, s40, 1\n"
    "s_cmp_lt_u32 s40, 262144\n"
    "s_cbranch_scc1 .Ls_63\n"
    "s_branch .Lb_64\n"
    ".Ls_65:\n"
    ".Lw_67:\n"
    "ds_read_b32 v3, v9 offset:64\n"
    "s_waitcnt lgkmcnt(0)\n"
    "v_readfirstlane_b32 s48, v3\n"
    "s_cmp_ge_u32 s48, 1\n"
    "s_cbranch_scc1 .Lb_66\n"
    "s_add_u32 s40, s40, 1\n"
    "s_cmp_lt_u32 s40, 262144\n"
    "s_cbranch_scc1 .Lw_67\n"
    "s_branch .Lb_66\n"
    ".Ls_68:\n"
    ".Lw_70:\n"
    "ds_read_b32 v3, v9 offset:64\n"
    "s_waitcnt lgkmcnt(0)\n"
    "v_readfirstlane_b32 s48, v3\n"
    "s_cmp_ge_u32 s48, 1\n"
    "s_cbranch_scc1 .Lb_69\n"
    "s_add_u32 s40, s40, 1\n"
    "s_cmp_lt_u32 s40, 262144\n"
    "s_cbranch_scc1 .Lw_70\n"
    "s_branch .Lb_69\n"
    ".LsubH:\n"
    "s_sleep 2\n"
    "buffer_load_dwordx4 v[44:47], v1, s[4:7], s12 offen sc1\n"
    "s_waitcnt vmcnt(0)\n"
    "s_setpc_b64 s[56:57]\n"
    ".LsubD0:\n"
    "s_sleep 2\n"
    "s_waitcnt vmcnt(0)\n"
    "v_max3_u32 v15, v63, v67, v71\n"
    "v_max_u32 v15, v15, v85\n"
    "v_cmp_le_u32_e32 vcc, 0xc0000000, v15\n"
    "s_cbranch_vccz .LsubD00\n"
    "buffer_load_dwordx4 v[60:63], v1, s[4:7], s8 offen offset:0 sc1\n"
    "buffer_load_dwordx4 v[64:67], v1, s[4:7], s8 offen offset:1024 sc1\n"
    "buffer_load_dwordx4 v[68:71], v1, s[4:7], s8 offen offset:2048 sc1\n"
    "buffer_load_dwordx2 v[84:85], v10, s[4:7], s8 offen offset:3072 sc1\n"
    ".LsubD00:\n"
    "v_max3_u32 v15, v75, v79, v83\n"
    "v_max_u32 v15, v15, v87\n"
    "v_cmp_le_u32_e32 vcc, 0xc0000000, v15\n"
    "s_cbranch_vccz .LsubD01\n"
    "buffer_load_dwordx4 v[72:75], v1, s[4:7], s9 offen offset:0 sc1\n"
    "buffer_load_dwordx4 v[76:79], v1, s[4:7], s9 offen offset:1024 sc1\n"
    "buffer_load_dwordx4 v[80:83], v1, s[4:7], s9 offen offset:2048 sc1\n"
    "buffer_load_dwordx2 v[86:87], v10, s[4:7], s9 offen offset:3072 sc1\n"
    ".LsubD01:\n"
    "v_max3_u32 v15, v19, v23, v27\n"
    "v_max_u32 v15, v15, v41\n"
    "v_cmp_le_u32_e32 vcc, 0xc0000000, v15\n"
    "s_cbranch_vccz .LsubD02\n"
    "buffer_load_dwordx4 v[16:19], v1, s[4:7], s10 offen offset:0 sc1\n"
    "buffer_load_dwordx4 v[20:23], v1, s[4:7], s10 offen offset:1024 sc1\n"
    "buffer_load_dwordx4 v[24:27], v1, s[4:7], s10 offen offset:2048 sc1\n"
    "buffer_load_dwordx2 v[40:41], v10, s[4:7], s10 offen offset:3072 sc1\n"
    ".LsubD02:\n"
    "v_max3_u32 v15, v31, v35, v39\n"
    "v_max_u32 v15, v15, v43\n"
    "v_cmp_le_u32_e32 vcc, 0xc0000000, v15\n"
    "s_cbranch_vccz .LsubD03\n"
    "buffer_load_dwordx4 v[28:31], v1, s[4:7], s11 offen offset:0 sc1\n"
    "buffer_load_dwordx4 v[32:35], v1, s[4:7], s11 offen offset:1024 sc1\n"
    "buffer_load_dwordx4 v[36:39], v1, s[4:7], s11 offen offset:2048 sc1\n"
    "buffer_load_dwordx2 v[42:43], v10, s[4:7], s11 offen offset:3072 sc1\n"
    ".LsubD03:\n"
    "s_waitcnt vmcnt(0)\n"
    "s_setpc_b64 s[56:57]\n"
    ".Ls_73:\n"
    "s_call_b64 s[56:57], .LsubD0\n"
    "v_max3_u32 v15, v63, v67, v71\n"
    "v_max_u32 v15, v15, v85\n"
    "v_cmp_le_u32_e32 vcc, 0xc0000000, v15\n"
    "s_cbranch_vccz .Lb_74\n"
    "s_add_u32 s40, s40, 1\n"
    "s_cmp_lt_u32 s40, 262144\n"
    "s_cbranch_scc1 .Ls_73\n"
    "s_branch .Lb_74\n"
    ".Ls_75:\n"
    "s_call_b64 s[56:57], .LsubH\n"
    "v_max3_u32 v15, v44, v45, v46\n"
    "v_max_u32 v15, v15, v47\n"
    "v_cmp_eq_u32_e32 vcc, -1, v15\n"
    "s_cbranch_vccz .Lb_76\n"
    "s_add_u32 s40, s40, 1\n"
    "s_cmp_lt_u32 s40, 262144\n"
    "s_cbranch_scc1 .Ls_75\n"
    "s_branch .Lb_76\n"
    ".LsubD1:\n"
    "s_sleep 2\n"
    "s_waitcnt vmcnt(0)\n"
    "v_max3_u32 v15, v75, v79, v83\n"
    "v_max_u32 v15, v15, v87\n"
    "v_cmp_le_u32_e32 vcc, 0xc0000000, v15\n"
    "s_cbranch_vccz .LsubD11\n"
    "buffer_load_dwordx4 v[72:75], v1, s[4:7], s9 offen offset:0 sc1\n"
    "buffer_load_dwordx4 v[76:79], v1, s[4:7], s9 offen offset:1024 sc1\n"
    "buffer_load_dwordx4 v[80:83], v1, s[4:7], s9 offen offset:2048 sc1\n"
    "buffer_load_dwordx2 v[86:87], v10, s[4:7], s9 offen offset:3072 sc1\n"
    ".LsubD11:\n"
    "v_max3_u32 v15, v19, v23, v27\n"
    "v_max_u32 v15, v15, v41\n"
    "v_cmp_le_u32_e32 vcc, 0xc0000000, v15\n"
    "s_cbranch_vccz .LsubD12\n"
    "buffer_load_dwordx4 v[16:19], v1, s[4:7], s10 offen offset:0 sc1\n"
    "buffer_load_dwordx4 v[20:23], v1, s[4:7], s10 offen offset:1024 sc1\n"
    "buffer_load_dwordx4 v[24:27], v1, s[4:7], s10 offen offset:2048 sc1\n"
    "buffer_load_dwordx2 v[40:41], v10, s[4:7], s10 offen offset:3072 sc1\n"
    ".LsubD12:\n"
    "v_max3_u32 v15, v31, v35, v39\n"
    "v_max_u32 v15, v15, v43\n"
    "v_cmp_le_u32_e32 vcc, 0xc0000000, v15\n"
    "s_cbranch_vccz .LsubD13\n"
    "buffer_load_dwordx4 v[28:31], v1, s[4:7], s11 offen offset:0 sc1\n"
    "buffer_load_dwordx4 v[32:35], v1, s[4:7], s11 offen offset:1024 sc1\n"
    "buffer_load_dwordx4 v[36:39], v1, s[4:7], s11 offen offset:2048 sc1\n"
    "buffer_load_dwordx2 v[42:43], v10, s[4:7], s11 offen offset:3072 sc1\n"
    ".LsubD13:\n"
    "s_waitcnt vmcnt(0)\n"
    "s_setpc_b64 s[56:57]\n"
    ".Ls_77:\n"
    "s_call_b64 s[56:57], .LsubD1\n"
    "v_max3_u32 v15, v75, v79, v83\n"
    "v_max_u32 v15, v15, v87\n"
    "v_cmp_le_u32_e32 vcc, 0xc0000000, v15\n"
    "s_cbranch_vccz .Lb_78\n"
    "s_add_u32 s40, s40, 1\n"
    "s_cmp_lt_u32 s40, 262144\n"
    "s_cbranch_scc1 .Ls_77\n"
    "s_branch .Lb_78\n"
    ".Ltw2:\n"
    "s_memrealtime s[50:51]\n"
    "s_waitcnt lgkmcnt(0)\n"
    "s_sub_u32 s50, s50, s52\n"
    "s_cmp_lt_u32 s50, 110\n"
    "s_cbranch_scc1 .Ltw2\n"
    "s_branch .Ltw3\n"
    ".LsubD2:\n"
    "s_sleep 2\n"
    "s_waitcnt vmcnt(0)\n"
    "v_max3_u32 v15, v19, v23, v27\n"
    "v_max_u32 v15, v15, v41\n"
    "v_cmp_le_u32_e32 vcc, 0xc0000000, v15\n"
    "s_cbranch_vccz .LsubD22\n"
    "buffer_load_dwordx4 v[16:19], v1, s[4:7], s10 offen offset:0 sc1\n"
    "buffer_load_dwordx4 v[20:23], v1, s[4:7], s10 offen offset:1024 sc1\n"
    "buffer_load_dwordx4 v[24:27], v1, s[4:7], s10 offen offset:2048 sc1\n"
    "buffer_load_dwordx2 v[40:41], v10, s[4:7], s10 offen offset:3072 sc1\n"
    ".LsubD22:\n"
    "v_max3_u32 v15, v31, v35, v39\n"
    "v_max_u32 v15, v15, v43\n"
    "v_cmp_le_u32_e32 vcc, 0xc0000000, v15\n"
    "s_cbranch_vccz .LsubD23\n"
    "buffer_load_dwordx4 v[28:31], v1, s[4:7], s11 offen offset:0 sc1\n"
    "buffer_load_dwordx4 v[32:35], v1, s[4:7], s11 offen offset:1024 sc1\n"
    "buffer_load_dwordx4 v[36:39], v1, s[4:7], s11 offen offset:2048 sc1\n"
    "buffer_load_dwordx2 v[42:43], v10, s[4:7], s11 offen offset:3072 sc1\n"
    ".LsubD23:\n"
    "s_waitcnt vmcnt(0)\n"
    "s_setpc_b64 s[56:57]\n"
    ".Ls_79:\n"
    "s_call_b64 s[56:57], .LsubD2\n"
    "v_max3_u32 v15, v19, v23, v27\n"
    "v_max_u32 v15, v15, v41\n"
    "v_cmp_le_u32_e32 vcc, 0xc0000000, v15\n"
    "s_cbranch_vccz .Lb_80\n"
    "s_add_u32 s40, s40, 1\n"
    "s_cmp_lt_u32 s40, 262144\n"
    "s_cbranch_scc1 .Ls_79\n"
    "s_branch .Lb_80\n"
    ".LsubD3:\n"
    "s_sleep 2\n"
    "s_waitcnt vmcnt(0)\n"
    "v_max3_u32 v15, v31, v35, v39\n"
    "v_max_u32 v15, v15, v43\n"
    "v_cmp_le_u32_e32 vcc, 0xc0000000, v15\n"
    "s_cbranch_vccz .LsubD33\n"
    "buffer_load_dwordx4 v[28:31], v1, s[4:7], s11 offen offset:0 sc1\n"
    "buffer_load_dwordx4 v[32:35], v1, s[4:7], s11 offen offset:1024 sc1\n"
    "buffer_load_dwordx4 v[36:39], v1, s[4:7], s11 offen offset:2048 sc1\n"
    "buffer_load_dwordx2 v[42:43], v10, s[4:7], s11 offen offset:3072 sc1\n"
    ".LsubD33:\n"
    "s_waitcnt vmcnt(0)\n"
    "s_setpc_b64 s[56:57]\n"
    ".Ls_81:\n"
    "s_call_b64 s[56:57], .LsubD3\n"
    "v_max3_u32 v15, v31, v35, v39\n"
    "v_max_u32 v15, v15, v43\n"
    "v_cmp_le_u32_e32 vcc, 0xc0000000, v15\n"
    "s_cbranch_vccz .Lb_82\n"
    "s_add_u32 s40, s40, 1\n"
    "s_cmp_lt_u32 s40, 262144\n"
    "s_cbranch_scc1 .Ls_81\n"
    "s_branch .Lb_82\n"
    ".LsubD4:\n"
    "s_sleep 2\n"
    "s_waitcnt vmcnt(0)\n"
    "v_max3_u32 v15, v123, v127, v131\n"
    "v_max_u32 v15, v15, v45\n"
    "v_cmp_le_u32_e32 vcc, 0xc0000000, v15\n"
    "s_cbranch_vccz .LsubD44\n"
    "buffer_load_dwordx4 v[120:123], v1, s[4:7], s16 offen offset:0 sc1\n"
    "buffer_load_dwordx4 v[124:127], v1, s[4:7], s16 offen offset:1024 sc1\n"
    "buffer_load_dwordx4 v[128:131], v1, s[4:7], s16 offen offset:2048 sc1\n"
    "buffer_load_dwordx2 v[44:45], v10, s[4:7], s16 offen offset:3072 sc1\n"
    ".LsubD44:\n"
    "v_max3_u32 v15, v135, v139, v143\n"
    "v_max_u32 v15, v15, v47\n"
    "v_cmp_le_u32_e32 vcc, 0xc0000000, v15\n"
    "s_cbranch_vccz .LsubD45\n"
    "buffer_load_dwordx4 v[132:135], v1, s[4:7], s17 offen offset:0 sc1\n"
    "buffer_load_dwordx4 v[136:139], v1, s[4:7], s17 offen offset:1024 sc1\n"
    "buffer_load_dwordx4 v[140:143], v1, s[4:7], s17 offen offset:2048 sc1\n"
    "buffer_load_dwordx2 v[46:47], v10, s[4:7], s17 offen offset:3072 sc1\n"
    ".LsubD45:\n"
    "v_max3_u32 v15, v63, v67, v71\n"
    "v_max_u32 v15, v15, v85\n"
    "v_cmp_le_u32_e32 vcc, 0xc0000000, v15\n"
    "s_cbranch_vccz .LsubD46\n"
    "buffer_load_dwordx4 v[60:63], v1, s[4:7], s18 offen offset:0 sc1\n"
    "buffer_load_dwordx4 v[64:67], v1, s[4:7], s18 offen offset:1024 sc1\n"
    "buffer_load_dwordx4 v[68:71], v1, s[4:7], s18 offen offset:2048 sc1\n"
    "buffer_load_dwordx2 v[84:85], v10, s[4:7], s18 offen offset:3072 sc1\n"
    ".LsubD46:\n"
    "s_waitcnt vmcnt(0)\n"
    "s_setpc_b64 s[56:57]\n"
    ".Ls_83:\n"
    "s_call_b64 s[56:57], .LsubD4\n"
    "v_max3_u32 v15, v123, v127, v131\n"
    "v_max_u32 v15, v15, v45\n"
    "v_cmp_le_u32_e32 vcc, 0xc0000000, v15\n"
    "s_cbranch_vccz .Lb_84\n"
    "s_add_u32 s40, s40, 1\n"
    "s_cmp_lt_u32 s40, 262144\n"
    "s_cbranch_scc1 .Ls_83\n"
    "s_branch .Lb_84\n"
    ".LsubD5:\n"
    "s_sleep 2\n"
    "s_waitcnt vmcnt(0)\n"
    "v_max3_u32 v15, v135, v139, v143\n"
    "v_max_u32 v15, v15, v47\n"
    "v_cmp_le_u32_e32 vcc, 0xc0000000, v15\n"
    "s_cbranch_vccz .LsubD55\n"
    "buffer_load_dwordx4 v[132:135], v1, s[4:7], s17 offen offset:0 sc1\n"
    "buffer_load_dwordx4 v[136:139], v1, s[4:7], s17 offen offset:1024 sc1\n"
    "buffer_load_dwordx4 v[140:143], v1, s[4:7], s17 offen offset:2048 sc1\n"
    "buffer_load_dwordx2 v[46:47], v10, s[4:7], s17 offen offset:3072 sc1\n"
    ".LsubD55:\n"
    "v_max3_u32 v15, v63, v67, v71\n"
    "v_max_u32 v15, v15, v85\n"
    "v_cmp_le_u32_e32 vcc, 0xc0000000, v15\n"
    "s_cbranch_vccz .LsubD56\n"
    "buffer_load_dwordx4 v[60:63], v1, s[4:7], s18 offen offset:0 sc1\n"
    "buffer_load_dwordx4 v[64:67], v1, s[4:7], s18 offen offset:1024 sc1\n"
    "buffer_load_dwordx4 v[68:71], v1, s[4:7], s18 offen offset:2048 sc1\n"
    "buffer_load_dwordx2 v[84:85], v10, s[4:7], s18 offen offset:3072 sc1\n"
    ".LsubD56:\n"
    "v_max3_u32 v15, v75, v79, v83\n"
    "v_max_u32 v15, v15, v87\n"
    "v_cmp_le_u32_e32 vcc, 0xc0000000, v15\n"
    "s_cbranch_vccz .LsubD57\n"
    "buffer_load_dwordx4 v[72:75], v1, s[4:7], s19 offen offset:0 sc1\n"
    "buffer_load_dwordx4 v[76:79], v1, s[4:7], s19 offen offset:1024 sc1\n"
    "buffer_load_dwordx4 v[80:83], v1, s[4:7], s19 offen offset:2048 sc1\n"
    "buffer_load_dwordx2 v[86:87], v10, s[4:7], s19 offen offset:3072 sc1\n"
    ".LsubD57:\n"
    "s_waitcnt vmcnt(0)\n"
    "s_setpc_b64 s[56:57]\n"
    ".Ls_85:\n"
    "s_call_b64 s[56:57], .LsubD5\n"
    "v_max3_u32 v15, v135, v139, v143\n"
    "v_max_u32 v15, v15, v47\n"
    "v_cmp_le_u32_e32 vcc, 0xc0000000, v15\n"
    "s_cbranch_vccz .Lb_86\n"
    "s_add_u32 s40, s40, 1\n"
    "s_cmp_lt_u32 s40, 262144\n"
    "s_cbranch_scc1 .Ls_85\n"
    "s_branch .Lb_86\n"
    ".LsubD6:\n"
    "s_sleep 2\n"
    "s_waitcnt vmcnt(0)\n"
    "v_max3_u32 v15, v63, v67, v71\n"
    "v_max_u32 v15, v15, v85\n"
    "v_cmp_le_u32_e32 vcc, 0xc0000000, v15\n"
    "s_cbranch_vccz .LsubD66\n"
    "buffer_load_dwordx4 v[60:63], v1, s[4:7], s18 offen offset:0 sc1\n"
    "buffer_load_dwordx4 v[64:67], v1, s[4:7], s18 offen offset:1024 sc1\n"
    "buffer_load_dwordx4 v[68:71], v1, s[4:7], s18 offen offset:2048 sc1\n"
    "buffer_load_dwordx2 v[84:85], v10, s[4:7], s18 offen offset:3072 sc1\n"
    ".LsubD66:\n"
    "v_max3_u32 v15, v75, v79, v83\n"
    "v_max_u32 v15, v15, v87\n"
    "v_cmp_le_u32_e32 vcc, 0xc0000000, v15\n"
    "s_cbranch_vccz .LsubD67\n"
    "buffer_load_dwordx4 v[72:75], v1, s[4:7], s19 offen offset:0 sc1\n"
    "buffer_load_dwordx4 v[76:79], v1, s[4:7], s19 offen offset:1024 sc1\n"
    "buffer_load_dwordx4 v[80:83], v1, s[4:7], s19 offen offset:2048 sc1\n"
    "buffer_load_dwordx2 v[86:87], v10, s[4:7], s19 offen offset:3072 sc1\n"
    ".LsubD67:\n"
    "s_waitcnt vmcnt(0)\n"
    "s_setpc_b64 s[56:57]\n"
    ".Ls_87:\n"
    "s_call_b64 s[56:57], .LsubD6\n"
    "v_max3_u32 v15, v63, v67, v71\n"
    "v_max_u32 v15, v15, v85\n"
    "v_cmp_le_u32_e32 vcc, 0xc0000000, v15\n"
    "s_cbranch_vccz .Lb_88\n"
    "s_add_u32 s40, s40, 1\n"
    "s_cmp_lt_u32 s40, 262144\n"
    "s_cbranch_scc1 .Ls_87\n"
    "s_branch .Lb_88\n"
    ".Ls_89:\n"
    ".Lw_91:\n"
    "ds_read_b32 v3, v2 offset:60\n"
    "s_waitcnt lgkmcnt(0)\n"
    "v_readfirstlane_b32 s48, v3\n"
    "s_cmp_ge_u32 s48, 8\n"
    "s_cbranch_scc1 .Lb_90\n"
    "s_add_u32 s40, s40, 1\n"
    "s_cmp_lt_u32 s40, 262144\n"
    "s_cbranch_scc1 .Lw_91\n"
    "s_branch .Lb_90\n"
    ".LsubD7:\n"
    "s_sleep 2\n"
    "s_waitcnt vmcnt(0)\n"
    "v_max3_u32 v15, v75, v79, v83\n"
    "v_max_u32 v15, v15, v87\n"
    "v_cmp_le_u32_e32 vcc, 0xc0000000, v15\n"
    "s_cbranch_vccz .LsubD77\n"
    "buffer_load_dwordx4 v[72:75], v1, s[4:7], s19 offen offset:0 sc1\n"
    "buffer_load_dwordx4 v[76:79], v1, s[4:7], s19 offen offset:1024 sc1\n"
    "buffer_load_dwordx4 v[80:83], v1, s[4:7], s19 offen offset:2048 sc1\n"
    "buffer_load_dwordx2 v[86:87], v10, s[4:7], s19 offen offset:3072 sc1\n"
    ".LsubD77:\n"
    "s_waitcnt vmcnt(0)\n"
    "s_setpc_b64 s[56:57]\n"
    ".Ls_92:\n"
    "s_call_b64 s[56:57], .LsubD7\n"
    "v_max3_u32 v15, v75, v79, v83\n"
    "v_max_u32 v15, v15, v87\n"
    "v_cmp_le_u32_e32 vcc, 0xc0000000, v15\n"
    "s_cbranch_vccz .Lb_93\n"
    "s_add_u32 s40, s40, 1\n"
    "s_cmp_lt_u32 s40, 262144\n"
    "s_cbranch_scc1 .Ls_92\n"
    "s_branch .Lb_93\n"
    ".Lcode_b:\n"
    :: "s"(__builtin_amdgcn_kernarg_segment_ptr()), "s"(__builtin_amdgcn_workgroup_id_x()), "v"(__builtin_amdgcn_workitem_id_x())
    : "v1", "v2", "v3", "v4", "v5", "v6", "v7", "v8", "v9", "v10", "v11", "v12", "v13", "v14", "v15", "v16", "v17", "v18", "v19", "v20", "v21", "v22", "v23", "v24", "v25", "v26", "v27", "v28", "v29", "v30", "v31", "v32", "v33", "v34", "v35", "v36", "v37", "v38", "v39", "v40", "v41", "v42", "v43", "v44", "v45", "v46", "v47", "v48", "v49", "v50", "v51", "v52", "v53", "v54", "v55", "v56", "v57", "v58", "v59", "v60", "v61", "v62", "v63", "v64", "v65", "v66", "v67", "v68", "v69", "v70", "v71", "v72", "v73", "v74", "v75", "v76", "v77", "v78", "v79", "v80", "v81", "v82", "v83", "v84", "v85", "v86", "v87", "v88", "v89", "v90", "v91", "v92", "v93", "v94", "v95", "v96", "v97", "v98", "v99", "v100", "v101", "v102", "v103", "v104", "v105", "v106", "v107", "v108", "v109", "v110", "v111", "v112", "v113", "v114", "v115", "v116", "v117", "v118", "v119", "v120", "v121", "v122", "v123", "v124", "v125", "v126", "v127", "v128", "v129", "v130", "v131", "v132", "v133", "v134", "v135", "v136", "v137", "v138", "v139", "v140", "v141", "v142", "v143", "v144", "v145", "v146", "v147", "v148", "v149", "v150", "v151", "v152", "v153", "v154", "v155", "v156", "v157", "v158", "v159", "v160", "v161", "v162", "v163", "v164", "v165", "v166", "v167", "v168", "v169", "v170", "v171", "v172", "v173", "v174", "v175", "v176", "v177", "v178", "v179", "v180", "v181", "v182", "v183", "v184", "v185", "v186", "v187", "v188", "v189", "v190", "v191", "v192", "v193", "v194", "v195", "v196", "v197", "v198", "v199", "v200", "v201", "v202", "v203", "v204", "v205", "v206", "v207", "v208", "v209", "v210", "v211", "v212", "v213", "v214", "v215", "v216", "v217", "v218", "v219", "v220", "v221", "v222", "v223", "v224", "v225", "v226", "v227", "v228", "v229", "v230", "v231", "v232", "v233", "v234", "v235", "v236", "v237", "v238", "v239", "v240", "v241", "v242", "v243", "v244", "v245", "v246", "v247", "v248", "v249", "v250", "v251", "v252", "v253", "v254", "v255", "s4", "s5", "s6", "s7", "s8", "s9", "s10", "s11", "s12", "s13", "s14", "s15", "s16", "s17", "s18", "s19", "s20", "s21", "s22", "s23", "s24", "s25", "s26", "s27", "s28", "s29", "s30", "s31", "s32", "s33", "s34", "s35", "s36", "s37", "s38", "s39", "s40", "s41", "s42", "s43", "s44", "s45", "s46", "s47", "s48", "s49", "s50", "s51", "s52", "s53", "s54", "s55", "s56", "s57", "s58", "s59", "s60", "s61", "s62", "s63", "s64", "s65", "s66", "s67", "s68", "s69", "s70", "s71", "s72", "s73", "s74", "s75", "s76", "s77", "s78", "s79", "s80", "s81", "s82", "s83", "s84", "s85", "s86", "s87", "s88", "s89", "s90", "s91", "s92", "s93", "s94", "s95", "s96", "s97", "s98", "s99", "s100", "s101", "vcc", "m0", "memory");
#endif
}

extern "C" void kernel_launch(void* const* d_in, const int* in_sizes, int n_in, void* d_out, int out_size, void* d_ws, size_t ws_size, hipStream_t stream) {
  (void)hipFuncSetAttribute((const void*)fused_kernel, hipFuncAttributeMaxDynamicSharedMemorySize, 163840);
  fused_kernel<<<256, 512, 163840, stream>>>((const char*)d_in[1], (const char*)d_in[2], (const char*)d_in[0],
      (const char*)d_in[3], (char*)d_ws, (const char*)d_in[4], (const char*)d_in[7], (const char*)d_in[8],
      (const char*)d_in[5], (char*)d_out);
}
```
